```python
import torch

def _softmax(x, axis=-1):
    return torch.softmax(x, dim=axis)
def _gelu(x):
    return torch.nn.functional.gelu(x, approximate="tanh")
def _silu(x):
    return torch.nn.functional.silu(x)
def _sigmoid(x):
    return torch.sigmoid(x)
def _layernorm(x, axis=-1, eps=1e-5):
    m = x.mean(dim=axis, keepdim=True)
    v = x.var(dim=axis, keepdim=True, unbiased=False)
    return (x - m) / torch.sqrt(v + eps)
def _scatter_add(tgt, idx, src, axis=0):
    return tgt.clone().index_add_(axis, idx, src)
def _segment_sum(src, seg, n):
    out = torch.zeros((n,) + src.shape[1:], dtype=src.dtype)
    return out.index_add_(0, seg, src)
def _pad(x, pw):
    flat = tuple(v for pair in reversed(pw) for v in pair)
    return torch.nn.functional.pad(x, flat)

kernel_name = 'standalone__glu_gated_swiglu__prompt'

def setup_inputs():

    _w = (lambda a: (lambda t: t.to(torch.bfloat16) if t.is_floating_point() else t)(torch.as_tensor(a)))
    B, S, D, F = 1, 2048, 512, 1376
    return {
        "x": _w((lambda *s: torch.randn(*s, dtype=torch.bfloat16) * 0.05)(B, S, D)),
        "w1": _w((lambda *s: torch.randn(*s, dtype=torch.bfloat16) * 0.05)(D, F) * (2.0 / D) ** 0.5),
        "w3": _w((lambda *s: torch.randn(*s, dtype=torch.bfloat16) * 0.05)(D, F) * (2.0 / D) ** 0.5),
        "w2": _w((lambda *s: torch.randn(*s, dtype=torch.bfloat16) * 0.05)(F, D) * (2.0 / F) ** 0.5),
    }

def reference(x, w1, w3, w2):
    gate = torch.einsum("bsd,df->bsf", x, w1)
    up = torch.einsum("bsd,df->bsf", x, w3)
    h = _silu(gate) * up
    out = torch.einsum("bsf,fd->bsd", h, w2)
    return out
```

```cpp
#include <hip/hip_runtime.h>
#include <hip/hip_bf16.h>
#include <stdint.h>

typedef short bf16x8 __attribute__((ext_vector_type(8)));
typedef short s16x4 __attribute__((ext_vector_type(4)));
typedef float f32x16 __attribute__((ext_vector_type(16)));
typedef float f32x4 __attribute__((ext_vector_type(4)));
typedef unsigned u32x4 __attribute__((ext_vector_type(4)));
typedef __bf16 bf16x2_t __attribute__((ext_vector_type(2)));
typedef float f32x2 __attribute__((ext_vector_type(2)));
#define LAS __attribute__((address_space(3)))
#define DI __device__ __forceinline__

constexpr int S = 2048, D = 512, F = 1376;
constexpr int NFB = F / 16;

DI void glds16(unsigned voff, __amdgpu_buffer_rsrc_t rsrc, unsigned soff, unsigned ldsdst) {
  asm volatile("s_mov_b32 m0, %1\n\ts_nop 0\n\tbuffer_load_dwordx4 %0, %2, %3 offen lds"
               :: "v"(voff), "s"(ldsdst), "s"(rsrc), "s"(soff) : "memory");
}
DI bf16x8 lds_b128(unsigned a) { return *(LAS bf16x8*)a; }
DI bf16x8 lds_tr2(unsigned a0, unsigned a1) {
  s16x4 lo = __builtin_amdgcn_ds_read_tr16_b64_v4i16((LAS s16x4*)a0);
  s16x4 hi = __builtin_amdgcn_ds_read_tr16_b64_v4i16((LAS s16x4*)a1);
  return __builtin_shufflevector(lo, hi, 0, 1, 2, 3, 4, 5, 6, 7);
}
DI unsigned pack_bf16(float a, float b) {
  f32x2 v = {a, b};
  bf16x2_t r = __builtin_convertvector(v, bf16x2_t);
  return __builtin_bit_cast(unsigned, r);
}
DI float silu_mul(float g, float u) {
  float e = __builtin_amdgcn_exp2f(g * -1.44269504088896340736f);
  return g * u * __builtin_amdgcn_rcpf(1.0f + e);
}
#define MFMA32(a, b, c) __builtin_amdgcn_mfma_f32_32x32x16_bf16((a), (b), (c), 0, 0, 0)
#define WAITV(n) asm volatile("s_waitcnt vmcnt(" #n ")" ::: "memory")
#define BAR() do { asm volatile("s_waitcnt lgkmcnt(0)" ::: "memory"); __builtin_amdgcn_s_barrier(); asm volatile("" ::: "memory"); } while (0)

constexpr int P1_STAGE = 40960, P1_XB = 16384;
__global__ __launch_bounds__(256, 1) void k_p1(const char* __restrict__ x, const char* __restrict__ w1,
                                               const char* __restrict__ w3, char* __restrict__ hbuf) {
  extern __shared__ __attribute__((aligned(16))) char smem[];
  const int lane = threadIdx.x & 63;
  const int w = __builtin_amdgcn_readfirstlane(threadIdx.x >> 6);
  const int b = blockIdx.x, xcd = b & 7, idx = b >> 3;
  const int mt = 8 * (xcd & 1) + (idx & 7);
  const int fs = 4 * (xcd >> 1) + (idx >> 3);
  if (fs >= 15) return;
  auto rx = __builtin_amdgcn_make_buffer_rsrc((void*)x, 0, S * D * 2, 0x00020000);
  auto r1 = __builtin_amdgcn_make_buffer_rsrc((void*)w1, 0, D * F * 2, 0x00020000);
  auto r3 = __builtin_amdgcn_make_buffer_rsrc((void*)w3, 0, D * F * 2, 0x00020000);
  const unsigned xvo = (unsigned)((mt * 128 + w * 8 + (lane >> 3)) * 1024 + (((lane & 7) ^ ((4 * w + (lane >> 4)) & 7)) * 16));
  const unsigned wvo1 = (unsigned)((w * 16 + (lane >> 2)) * (F * 2) + fs * 192 + (lane & 3) * 16);
  const unsigned wvo3 = (unsigned)((w * 16 + (lane >> 2)) * (F * 2) + fs * 192 + ((lane & 3) ^ 2) * 16);
  auto issue = [&](int st) {
    const unsigned base = (st & 3) * P1_STAGE;
#pragma unroll
    for (int i = 0; i < 4; ++i) glds16(xvo, rx, st * 128 + i * 32768, base + (w + 4 * i) * 1024);
#pragma unroll
    for (int p = 0; p < 3; ++p) glds16(wvo1, r1, st * 64 * (F * 2) + p * 64, base + P1_XB + (w * 3 + p) * 1024);
#pragma unroll
    for (int p = 0; p < 3; ++p) glds16(wvo3, r3, st * 64 * (F * 2) + p * 64, base + P1_XB + 12288 + (w * 3 + p) * 1024);
  };
  issue(0); issue(1); issue(2);
  const int wm = w & 1, wf = w >> 1;
  const int t = lane & 31, h = lane >> 5, g = lane >> 4, m = g & 1, q = (lane >> 2) & 3, p = lane & 3;
  const unsigned xa0 = (unsigned)((wm * 64 + t) * 128 + ((h ^ ((t >> 1) & 7)) * 16));
  unsigned wa[3];
#pragma unroll
  for (int i = 0; i < 3; ++i) {
    const int ft = wf * 3 + i;
    wa[i] = (unsigned)(P1_XB + m * 12288 + (ft >> 1) * 1024 + h * 512 + q * 64 + (((ft & 1) ^ m) * 32) + p * 8);
  }
  f32x16 acc[2][3];
#pragma unroll
  for (int i = 0; i < 2; ++i)
#pragma unroll
    for (int j = 0; j < 3; ++j)
#pragma unroll
      for (int r = 0; r < 16; ++r) acc[i][j][r] = 0.f;
#pragma unroll
  for (int st = 0; st < 8; ++st) {
    if (st < 6) WAITV(20); else if (st == 6) WAITV(10); else WAITV(0);
    BAR();
    if (st + 3 < 8) issue(st + 3);
    const unsigned base = (st & 3) * P1_STAGE;
#pragma unroll
    for (int j = 0; j < 4; ++j) {
      bf16x8 xf[2], wfr[3];
#pragma unroll
      for (int i = 0; i < 2; ++i) xf[i] = lds_b128(base + (xa0 ^ (j * 32)) + i * 4096);
#pragma unroll
      for (int i = 0; i < 3; ++i) wfr[i] = lds_tr2(base + wa[i] + j * 3072, base + wa[i] + j * 3072 + 256);
#pragma unroll
      for (int i = 0; i < 2; ++i)
#pragma unroll
        for (int jj = 0; jj < 3; ++jj) acc[i][jj] = MFMA32(wfr[jj], xf[i], acc[i][jj]);
    }
  }
#pragma unroll
  for (int i = 0; i < 2; ++i)
#pragma unroll
    for (int jj = 0; jj < 3; ++jj) {
      const int fb = fs * 6 + wf * 3 + jj;
      const int tb = mt * 4 + wm * 2 + i;
      u32x4 v;
#pragma unroll
      for (int r = 0; r < 4; ++r)
        v[r] = pack_bf16(silu_mul(acc[i][jj][2 * r], acc[i][jj][8 + 2 * r]), silu_mul(acc[i][jj][2 * r + 1], acc[i][jj][8 + 2 * r + 1]));
      if (fb < NFB) *(u32x4*)(hbuf + (size_t)(tb * NFB + fb) * 1024 + lane * 16) = v;
    }
}

constexpr int P2_STAGE = 16384, P2_RED = 8 * P2_STAGE;
__global__ __launch_bounds__(256, 1) void k_p2(const char* __restrict__ hbuf, const char* __restrict__ w2, char* __restrict__ out) {
  extern __shared__ __attribute__((aligned(16))) char smem[];
  const int lane = threadIdx.x & 63;
  const int w = __builtin_amdgcn_readfirstlane(threadIdx.x >> 6);
  const int b = blockIdx.x, xcd = b & 7, idx = b >> 3;
  const int pb = xcd * 4 + (idx >> 3);
  const int nb = idx & 7;
  auto r2 = __builtin_amdgcn_make_buffer_rsrc((void*)w2, 0, F * D * 2, 0x00020000);
  auto rh = __builtin_amdgcn_make_buffer_rsrc((void*)hbuf, 0, 64 * NFB * 1024, 0x00020000);
  const int th = w & 1, kh = w >> 1;
  const unsigned wvo = (unsigned)((kh * 43 * 16 + th * 32 + (lane >> 3)) * 1024 + nb * 128 + (((lane & 7) ^ (((lane >> 4) & 1) << 2)) * 16));
  auto issue = [&](int st) {
    const unsigned base = (st & 7) * P2_STAGE + kh * 8192 + th * 4096;
#pragma unroll
    for (int i = 0; i < 4; ++i) glds16(wvo, r2, st * 65536 + i * 8192, base + i * 1024);
  };
  const unsigned hvo = (unsigned)(((pb * 2 + th) * NFB + kh * 43) * 1024 + lane * 16);
  u32x4 hf[43];
#pragma unroll
  for (int st = 0; st < 7; ++st) {
    issue(st);
#pragma unroll
    for (int j = 0; j < 4; ++j) asm volatile("buffer_load_dwordx4 %0, %1, %2, %3 offen" : "=v"(hf[st * 4 + j]) : "v"(hvo), "s"(rh), "s"((st * 4 + j) * 1024) : "memory");
  }
  const int h = lane >> 5, blk = (lane >> 4) & 1, q = (lane >> 2) & 3, p = lane & 3;
  unsigned wa[2];
#pragma unroll
  for (int u = 0; u < 2; ++u) wa[u] = (unsigned)(kh * 8192 + (4 * h + q) * 128 + ((u ^ ((q >> 1) & 1)) * 64) + blk * 32 + p * 8);
  f32x16 acc[2];
#pragma unroll
  for (int u = 0; u < 2; ++u)
#pragma unroll
    for (int r = 0; r < 16; ++r) acc[u][r] = 0.f;
#pragma unroll
  for (int st = 0; st < 11; ++st) {
    if (st <= 3) WAITV(48); else if (st == 4) WAITV(47); else if (st == 5) WAITV(39); else if (st == 6) WAITV(31);
    else if (st == 7) WAITV(23); else if (st == 8) WAITV(15); else if (st == 9) WAITV(7); else WAITV(0);
    BAR();
#pragma unroll
    for (int j = 0; j < 4; ++j) if (st * 4 + j < 43) asm volatile("" : "+v"(hf[st * 4 + j]));
    if (st + 7 < 11) {
      issue(st + 7);
#pragma unroll
      for (int j = 0; j < 4; ++j) if ((st + 7) * 4 + j < 43)
        asm volatile("buffer_load_dwordx4 %0, %1, %2, %3 offen" : "=v"(hf[(st + 7) * 4 + j]) : "v"(hvo), "s"(rh), "s"(((st + 7) * 4 + j) * 1024) : "memory");
    }
    const unsigned base = (st & 7) * P2_STAGE;
#pragma unroll
    for (int j = 0; j < 4; ++j) {
      if (st * 4 + j < 43) {
        bf16x8 wf[2];
#pragma unroll
        for (int u = 0; u < 2; ++u) wf[u] = lds_tr2(base + wa[u] + j * 2048, base + wa[u] + j * 2048 + 1024);
        const bf16x8 hv = __builtin_bit_cast(bf16x8, hf[st * 4 + j]);
#pragma unroll
        for (int u = 0; u < 2; ++u) acc[u] = MFMA32(wf[u], hv, acc[u]);
      }
    }
  }
  const int t = lane & 31;
#pragma unroll
  for (int u = 0; u < 2; ++u)
#pragma unroll
    for (int g = 0; g < 4; ++g) {
      const int slot = u * 8 + g * 2 + h;
      f32x4 v = {acc[u][4 * g], acc[u][4 * g + 1], acc[u][4 * g + 2], acc[u][4 * g + 3]};
      *(LAS f32x4*)(unsigned)(P2_RED + kh * 16384 + (th * 32 + t) * 256 + ((slot ^ (t & 7)) * 16)) = v;
    }
  BAR();
#pragma unroll
  for (int i = 0; i < 2; ++i) {
    const int row = i * 32 + (threadIdx.x >> 3), c = threadIdx.x & 7;
    const unsigned a0 = P2_RED + row * 256 + (((2 * c) ^ (row & 7)) * 16), a1 = P2_RED + row * 256 + (((2 * c + 1) ^ (row & 7)) * 16);
    f32x4 v0 = *(LAS f32x4*)a0 + *(LAS f32x4*)(a0 + 16384);
    f32x4 v1 = *(LAS f32x4*)a1 + *(LAS f32x4*)(a1 + 16384);
    u32x4 o = {pack_bf16(v0[0], v0[1]), pack_bf16(v0[2], v0[3]), pack_bf16(v1[0], v1[1]), pack_bf16(v1[2], v1[3])};
    *(u32x4*)(out + (size_t)(pb * 64 + row) * 1024 + nb * 128 + c * 16) = o;
  }
}

extern "C" void kernel_launch(void* const* d_in, const int* in_sizes, int n_in, void* d_out, int out_size,
                              void* d_ws, size_t ws_size, hipStream_t stream) {
  hipFuncSetAttribute((const void*)k_p1, hipFuncAttributeMaxDynamicSharedMemorySize, 163840);
  hipFuncSetAttribute((const void*)k_p2, hipFuncAttributeMaxDynamicSharedMemorySize, 163840);
  k_p1<<<256, 256, 163840, stream>>>((const char*)d_in[0], (const char*)d_in[1], (const char*)d_in[2], (char*)d_ws);
  k_p2<<<256, 256, 163840, stream>>>((const char*)d_ws, (const char*)d_in[3], (char*)d_out);
}
```

```cpp
#include <hip/hip_runtime.h>
#include <hip/hip_bf16.h>
#include <stdint.h>

typedef short bf16x8 __attribute__((ext_vector_type(8)));
typedef short s16x4 __attribute__((ext_vector_type(4)));
typedef float f32x16 __attribute__((ext_vector_type(16)));
typedef float f32x4 __attribute__((ext_vector_type(4)));
typedef unsigned u32x4 __attribute__((ext_vector_type(4)));
typedef __bf16 bf16x2_t __attribute__((ext_vector_type(2)));
typedef float f32x2 __attribute__((ext_vector_type(2)));
#define LAS __attribute__((address_space(3)))
#define DI __device__ __forceinline__

constexpr int S = 2048, D = 512, F = 1376;
constexpr int NFB = F / 16;

DI void glds16(unsigned voff, __amdgpu_buffer_rsrc_t rsrc, unsigned soff, unsigned ldsdst) {
  asm volatile("s_mov_b32 m0, %1\n\ts_nop 0\n\tbuffer_load_dwordx4 %0, %2, %3 offen lds"
               :: "v"(voff), "s"(ldsdst), "s"(rsrc), "s"(soff) : "memory");
}
DI bf16x8 lds_b128(unsigned a) { return *(LAS bf16x8*)a; }
DI bf16x8 lds_tr2(unsigned a0, unsigned a1) {
  s16x4 lo = __builtin_amdgcn_ds_read_tr16_b64_v4i16((LAS s16x4*)a0);
  s16x4 hi = __builtin_amdgcn_ds_read_tr16_b64_v4i16((LAS s16x4*)a1);
  return __builtin_shufflevector(lo, hi, 0, 1, 2, 3, 4, 5, 6, 7);
}
DI unsigned pack_bf16(float a, float b) {
  f32x2 v = {a, b};
  bf16x2_t r = __builtin_convertvector(v, bf16x2_t);
  return __builtin_bit_cast(unsigned, r);
}
DI float silu_mul(float g, float u) {
  float e = __builtin_amdgcn_exp2f(g * -1.44269504088896340736f);
  return g * u * __builtin_amdgcn_rcpf(1.0f + e);
}
#define HAVE_STAMPS 1
DI unsigned long long rstamp() { unsigned long long t; asm volatile("s_memrealtime %0\n\ts_waitcnt lgkmcnt(0)" : "=s"(t) :: "memory"); return t; }
DI unsigned long long cstamp() { unsigned long long t; asm volatile("s_memtime %0\n\ts_waitcnt lgkmcnt(0)" : "=s"(t) :: "memory"); return t; }
#define STAMP(i) do { __builtin_amdgcn_sched_barrier(0); ts[i] = cstamp(); __builtin_amdgcn_sched_barrier(0); } while (0)
#define MFMA32(a, b, c) __builtin_amdgcn_mfma_f32_32x32x16_bf16((a), (b), (c), 0, 0, 0)
#define WAITV(n) asm volatile("s_waitcnt vmcnt(" #n ")" ::: "memory")
#define BAR() do { asm volatile("s_waitcnt lgkmcnt(0)" ::: "memory"); __builtin_amdgcn_s_barrier(); asm volatile("" ::: "memory"); } while (0)

constexpr int P1_STAGE = 40960, P1_XB = 16384;
__global__ __launch_bounds__(256, 1) void k_p1(const char* __restrict__ x, const char* __restrict__ w1,
                                               const char* __restrict__ w3, char* __restrict__ hbuf) {
  extern __shared__ __attribute__((aligned(16))) char smem[];
  unsigned long long ts[16]; unsigned long long r0 = rstamp(); STAMP(0);
  const int lane = threadIdx.x & 63;
  const int w = __builtin_amdgcn_readfirstlane(threadIdx.x >> 6);
  const int b = blockIdx.x, xcd = b & 7, idx = b >> 3;
  const int mt = 8 * (xcd & 1) + (idx & 7);
  const int fs = 4 * (xcd >> 1) + (idx >> 3);
  if (fs >= 15) return;
  auto rx = __builtin_amdgcn_make_buffer_rsrc((void*)x, 0, S * D * 2, 0x00020000);
  auto r1 = __builtin_amdgcn_make_buffer_rsrc((void*)w1, 0, D * F * 2, 0x00020000);
  auto r3 = __builtin_amdgcn_make_buffer_rsrc((void*)w3, 0, D * F * 2, 0x00020000);
  const unsigned xvo = (unsigned)((mt * 128 + w * 8 + (lane >> 3)) * 1024 + (((lane & 7) ^ ((4 * w + (lane >> 4)) & 7)) * 16));
  const unsigned wvo1 = (unsigned)((w * 16 + (lane >> 2)) * (F * 2) + fs * 192 + (lane & 3) * 16);
  const unsigned wvo3 = (unsigned)((w * 16 + (lane >> 2)) * (F * 2) + fs * 192 + ((lane & 3) ^ 2) * 16);
  auto issue = [&](int st) {
    const unsigned base = (st & 3) * P1_STAGE;
#pragma unroll
    for (int i = 0; i < 4; ++i) glds16(xvo, rx, st * 128 + i * 32768, base + (w + 4 * i) * 1024);
#pragma unroll
    for (int p = 0; p < 3; ++p) glds16(wvo1, r1, st * 64 * (F * 2) + p * 64, base + P1_XB + (w * 3 + p) * 1024);
#pragma unroll
    for (int p = 0; p < 3; ++p) glds16(wvo3, r3, st * 64 * (F * 2) + p * 64, base + P1_XB + 12288 + (w * 3 + p) * 1024);
  };
  issue(0); issue(1); issue(2);
  STAMP(1);
  const int wm = w & 1, wf = w >> 1;
  const int t = lane & 31, h = lane >> 5, g = lane >> 4, m = g & 1, q = (lane >> 2) & 3, p = lane & 3;
  const unsigned xa0 = (unsigned)((wm * 64 + t) * 128 + ((h ^ ((t >> 1) & 7)) * 16));
  unsigned wa[3];
#pragma unroll
  for (int i = 0; i < 3; ++i) {
    const int ft = wf * 3 + i;
    wa[i] = (unsigned)(P1_XB + m * 12288 + (ft >> 1) * 1024 + h * 512 + q * 64 + (((ft & 1) ^ m) * 32) + p * 8);
  }
  f32x16 acc[2][3];
#pragma unroll
  for (int i = 0; i < 2; ++i)
#pragma unroll
    for (int j = 0; j < 3; ++j)
#pragma unroll
      for (int r = 0; r < 16; ++r) acc[i][j][r] = 0.f;
#pragma unroll
  for (int st = 0; st < 8; ++st) {
    if (st < 6) WAITV(20); else if (st == 6) WAITV(10); else WAITV(0);
    BAR();
    STAMP(2 + st);
    if (st + 3 < 8) issue(st + 3);
    const unsigned base = (st & 3) * P1_STAGE;
#pragma unroll
    for (int j = 0; j < 4; ++j) {
      bf16x8 xf[2], wfr[3];
#pragma unroll
      for (int i = 0; i < 2; ++i) xf[i] = lds_b128(base + (xa0 ^ (j * 32)) + i * 4096);
#pragma unroll
      for (int i = 0; i < 3; ++i) wfr[i] = lds_tr2(base + wa[i] + j * 3072, base + wa[i] + j * 3072 + 256);
#pragma unroll
      for (int i = 0; i < 2; ++i)
#pragma unroll
        for (int jj = 0; jj < 3; ++jj) acc[i][jj] = MFMA32(wfr[jj], xf[i], acc[i][jj]);
    }
  }
  STAMP(10);
#pragma unroll
  for (int i = 0; i < 2; ++i)
#pragma unroll
    for (int jj = 0; jj < 3; ++jj) {
      const int fb = fs * 6 + wf * 3 + jj;
      const int tb = mt * 4 + wm * 2 + i;
      u32x4 v;
#pragma unroll
      for (int r = 0; r < 4; ++r)
        v[r] = pack_bf16(silu_mul(acc[i][jj][2 * r], acc[i][jj][8 + 2 * r]), silu_mul(acc[i][jj][2 * r + 1], acc[i][jj][8 + 2 * r + 1]));
      if (fb < NFB) *(u32x4*)(hbuf + (size_t)(tb * NFB + fb) * 1024 + lane * 16) = v;
    }
  STAMP(11);
  WAITV(0);
  STAMP(12);
  unsigned long long r1_ = rstamp();
  if (threadIdx.x == 0) { unsigned* o = (unsigned*)(hbuf + (16 << 20)) + blockIdx.x * 32; for (int i = 0; i < 13; ++i) o[i] = (unsigned)(ts[i] - ts[0]); o[30] = (unsigned)r0; o[31] = (unsigned)r1_; }
}

constexpr int P2_STAGE = 16384, P2_RED = 8 * P2_STAGE;
__global__ __launch_bounds__(256, 1) void k_p2(char* __restrict__ hbuf, const char* __restrict__ w2, char* __restrict__ out) {
  extern __shared__ __attribute__((aligned(16))) char smem[];
  unsigned long long ts[16]; unsigned long long r0 = rstamp(); STAMP(0);
  const int lane = threadIdx.x & 63;
  const int w = __builtin_amdgcn_readfirstlane(threadIdx.x >> 6);
  const int b = blockIdx.x, xcd = b & 7, idx = b >> 3;
  const int pb = xcd * 4 + (idx >> 3);
  const int nb = idx & 7;
  auto r2 = __builtin_amdgcn_make_buffer_rsrc((void*)w2, 0, F * D * 2, 0x00020000);
  auto rh = __builtin_amdgcn_make_buffer_rsrc((void*)hbuf, 0, 64 * NFB * 1024, 0x00020000);
  const int th = w & 1, kh = w >> 1;
  const unsigned wvo = (unsigned)((kh * 43 * 16 + th * 32 + (lane >> 3)) * 1024 + nb * 128 + (((lane & 7) ^ (((lane >> 4) & 1) << 2)) * 16));
  auto issue = [&](int st) {
    const unsigned base = (st & 7) * P2_STAGE + kh * 8192 + th * 4096;
#pragma unroll
    for (int i = 0; i < 4; ++i) glds16(wvo, r2, st * 65536 + i * 8192, base + i * 1024);
  };
  const unsigned hvo = (unsigned)(((pb * 2 + th) * NFB + kh * 43) * 1024 + lane * 16);
  u32x4 hf[43];
#pragma unroll
  for (int st = 0; st < 7; ++st) {
    issue(st);
#pragma unroll
    for (int j = 0; j < 4; ++j) asm volatile("buffer_load_dwordx4 %0, %1, %2, %3 offen" : "=v"(hf[st * 4 + j]) : "v"(hvo), "s"(rh), "s"((st * 4 + j) * 1024) : "memory");
  }
  STAMP(1);
  const int h = lane >> 5, blk = (lane >> 4) & 1, q = (lane >> 2) & 3, p = lane & 3;
  unsigned wa[2];
#pragma unroll
  for (int u = 0; u < 2; ++u) wa[u] = (unsigned)(kh * 8192 + (4 * h + q) * 128 + ((u ^ ((q >> 1) & 1)) * 64) + blk * 32 + p * 8);
  f32x16 acc[2];
#pragma unroll
  for (int u = 0; u < 2; ++u)
#pragma unroll
    for (int r = 0; r < 16; ++r) acc[u][r] = 0.f;
#pragma unroll
  for (int st = 0; st < 11; ++st) {
    if (st <= 3) WAITV(48); else if (st == 4) WAITV(47); else if (st == 5) WAITV(39); else if (st == 6) WAITV(31);
    else if (st == 7) WAITV(23); else if (st == 8) WAITV(15); else if (st == 9) WAITV(7); else WAITV(0);
    BAR();
    STAMP(2 + st);
#pragma unroll
    for (int j = 0; j < 4; ++j) if (st * 4 + j < 43) asm volatile("" : "+v"(hf[st * 4 + j]));
    if (st + 7 < 11) {
      issue(st + 7);
#pragma unroll
      for (int j = 0; j < 4; ++j) if ((st + 7) * 4 + j < 43)
        asm volatile("buffer_load_dwordx4 %0, %1, %2, %3 offen" : "=v"(hf[(st + 7) * 4 + j]) : "v"(hvo), "s"(rh), "s"(((st + 7) * 4 + j) * 1024) : "memory");
    }
    const unsigned base = (st & 7) * P2_STAGE;
#pragma unroll
    for (int j = 0; j < 4; ++j) {
      if (st * 4 + j < 43) {
        bf16x8 wf[2];
#pragma unroll
        for (int u = 0; u < 2; ++u) wf[u] = lds_tr2(base + wa[u] + j * 2048, base + wa[u] + j * 2048 + 1024);
        const bf16x8 hv = __builtin_bit_cast(bf16x8, hf[st * 4 + j]);
#pragma unroll
        for (int u = 0; u < 2; ++u) acc[u] = MFMA32(wf[u], hv, acc[u]);
      }
    }
  }
  STAMP(13);
  const int t = lane & 31;
#pragma unroll
  for (int u = 0; u < 2; ++u)
#pragma unroll
    for (int g = 0; g < 4; ++g) {
      const int slot = u * 8 + g * 2 + h;
      f32x4 v = {acc[u][4 * g], acc[u][4 * g + 1], acc[u][4 * g + 2], acc[u][4 * g + 3]};
      *(LAS f32x4*)(unsigned)(P2_RED + kh * 16384 + (th * 32 + t) * 256 + ((slot ^ (t & 7)) * 16)) = v;
    }
  BAR();
#pragma unroll
  for (int i = 0; i < 2; ++i) {
    const int row = i * 32 + (threadIdx.x >> 3), c = threadIdx.x & 7;
    const unsigned a0 = P2_RED + row * 256 + (((2 * c) ^ (row & 7)) * 16), a1 = P2_RED + row * 256 + (((2 * c + 1) ^ (row & 7)) * 16);
    f32x4 v0 = *(LAS f32x4*)a0 + *(LAS f32x4*)(a0 + 16384);
    f32x4 v1 = *(LAS f32x4*)a1 + *(LAS f32x4*)(a1 + 16384);
    u32x4 o = {pack_bf16(v0[0], v0[1]), pack_bf16(v0[2], v0[3]), pack_bf16(v1[0], v1[1]), pack_bf16(v1[2], v1[3])};
    *(u32x4*)(out + (size_t)(pb * 64 + row) * 1024 + nb * 128 + c * 16) = o;
  }
  STAMP(14);
  WAITV(0);
  STAMP(15);
  unsigned long long r1_ = rstamp();
  if (threadIdx.x == 0) { unsigned* o = (unsigned*)(hbuf + (17 << 20)) + blockIdx.x * 32; for (int i = 0; i < 16; ++i) o[i] = (unsigned)(ts[i] - ts[0]); o[30] = (unsigned)r0; o[31] = (unsigned)r1_; }
}

#define VALID(k, b) (((k) == 1 || (4 * (((b) & 7) >> 1) + ((b) >> 6)) < 15))

__global__ void k_spin(const char* ws, int k, int idx, int stat, int nvalid, unsigned* sink) {
  const unsigned* T0 = (const unsigned*)(ws + (16 << 20));
  const unsigned* T1 = (const unsigned*)(ws + (17 << 20));
  __shared__ unsigned v[256]; __shared__ unsigned res;
  int t = threadIdx.x;
  unsigned ticks = 0;
  if (stat < 3) {
    const unsigned* T = k ? T1 : T0;
    bool ok = VALID(k, t);
    v[t] = ok ? T[t * 32 + idx] : 0xffffffffu;
    if (t == 0) res = 0;
    __syncthreads();
    int rank = 0; for (int j = 0; j < 256; ++j) rank += (v[j] < v[t]) || (v[j] == v[t] && j < t);
    int want = stat == 0 ? nvalid / 2 : stat == 1 ? nvalid - 1 : 0;
    if (rank == want) res = v[t];
    __syncthreads();
    ticks = res * 2 / 5;
  } else {
    unsigned mn0 = ~0u, mx0 = 0, me0 = 0, mn1 = ~0u, mx1 = 0, me1 = 0;
    for (int j = 0; j < 256; ++j) {
      if (VALID(0, j)) { mn0 = min(mn0, T0[j * 32 + 30]); mx0 = max(mx0, T0[j * 32 + 30]); me0 = max(me0, T0[j * 32 + 31]); }
      if (VALID(1, j)) { mn1 = min(mn1, T1[j * 32 + 30]); mx1 = max(mx1, T1[j * 32 + 30]); me1 = max(me1, T1[j * 32 + 31]); }
    }
    unsigned x = idx == 0 ? me0 - mn0 : idx == 1 ? mn1 - me0 : idx == 2 ? me1 - mn1 : idx == 3 ? mx0 - mn0 : 0;
    ticks = x * 10;
  }
  if (ticks > 100000) ticks = 100000;
  unsigned long long r0 = rstamp();
  while (rstamp() - r0 < ticks) __builtin_amdgcn_s_sleep(1);
  if (ticks == 0x7fffffff) sink[0] = 1;
}

extern "C" void kernel_launch(void* const* d_in, const int* in_sizes, int n_in, void* d_out, int out_size,
                              void* d_ws, size_t ws_size, hipStream_t stream) {
  hipFuncSetAttribute((const void*)k_p1, hipFuncAttributeMaxDynamicSharedMemorySize, 163840);
  hipFuncSetAttribute((const void*)k_p2, hipFuncAttributeMaxDynamicSharedMemorySize, 163840);
  k_p1<<<256, 256, 163840, stream>>>((const char*)d_in[0], (const char*)d_in[1], (const char*)d_in[2], (char*)d_ws);
  k_p2<<<256, 256, 163840, stream>>>((char*)d_ws, (const char*)d_in[3], (char*)d_out);
  k_spin<<<1, 256, 0, stream>>>((const char*)d_ws, 0, 1, 0, 240, (unsigned*)d_ws + (32 << 20));
}
```

```cpp
#include <hip/hip_runtime.h>
#include <hip/hip_bf16.h>

__global__ __launch_bounds__(512, 2) void glu_fused(const void* x, const void* w1, const void* w3, const void* w2,
                                                    void* out, void* ws) {
  auto ka = __builtin_amdgcn_kernarg_segment_ptr();
  asm volatile(
      "s_load_dwordx8 s[8:15], %0, 0x0\n"
      "s_load_dwordx4 s[16:19], %0, 0x20\n"
      "v_lshrrev_b32 v3, 6, %2\n"
      "v_lshlrev_b32 v4, 7, %2\n"
      "v_readfirstlane_b32 s56, v3\n"
      "s_getpc_b64 s[94:95]\n"
      ".Lglu_CODE_PC:\n"
      "v_min_u32 v4, .Lglu_CODE_END-.Lglu_CODE_PC-4, v4\n"
      "global_load_dword v3, v4, s[94:95]\n"
      "s_mov_b32 s84, %1\n"
      "v_and_b32 v2, 63, %2\n"
      "s_and_b32 s70, s84, 7\n"
      "s_lshr_b32 s71, s84, 3\n"
      "s_sub_u32 s72, s71, 29\n"
      "s_cmp_lt_u32 s72, 2\n"
      "s_cselect_b32 s72, 3, 0\n"
      "s_xor_b32 s71, s71, s72\n"
      "s_mul_i32 s72, s70, 5\n"
      "s_mul_i32 s73, s70, 6\n"
      "s_sub_u32 s73, s73, 30\n"
      "s_sub_u32 s74, s71, 12\n"
      "s_cmp_eq_u32 s70, 4\n"
      "s_cselect_b32 s73, 18, s73\n"
      "s_cselect_b32 s74, s74, s71\n"
      "s_cselect_b32 s75, 32, 30\n"
      "s_lshl_b32 s76, s70, 5\n"
      "s_add_u32 s76, s76, s71\n"
      "s_cmp_ge_u32 s76, 140\n"
      "s_cselect_b32 s72, s73, s72\n"
      "s_cselect_b32 s74, s74, s71\n"
      "s_cselect_b32 s77, 52, 43\n"
      "s_cselect_b32 s78, 5, 6\n"
      "s_cselect_b32 s79, 6, 0\n"
      "s_cselect_b32 s80, 1, 0\n"
      "s_mul_i32 s77, s74, s77\n"
      "s_lshr_b32 s77, s77, 8\n"
      "s_add_u32 s57, s72, s77\n"
      "s_mul_i32 s77, s77, s78\n"
      "s_sub_u32 s77, s74, s77\n"
      "s_add_u32 s58, s79, s77\n"
      "s_cmp_lt_u32 s71, s75\n"
      "s_cselect_b32 s67, -1, 0\n"
      "s_cmp_ge_u32 s57, 7\n"
      "s_cselect_b32 s59, 0x12, 0x24\n"
      "s_cmp_ge_u32 s57, 14\n"
      "s_cselect_b32 s59, 0x09, s59\n"
      "s_mul_i32 s77, s58, 3\n"
      "s_and_b32 s78, s59, 3\n"
      "s_add_u32 s61, s77, s78\n"
      "s_cmp_ge_u32 s56, 4\n"
      "s_cbranch_scc1 .Lglu_C_ENTRY\n"
      "s_add_u32 s77, s58, s57\n"
      "s_and_b32 s77, s77, 3\n"
      "s_lshl_b32 s77, s77, 1\n"
      "s_add_u32 s45, s77, s80\n"
      "v_lshrrev_b32 v11, 3, v2\n"
      "v_and_b32 v12, 7, v2\n"
      "v_lshrrev_b32 v13, 4, v2\n"
      "v_and_b32 v14, 1, v13\n"
      "v_lshlrev_b32 v14, 2, v14\n"
      "v_xor_b32 v14, v14, v12\n"
      "s_lshl_b32 s70, s56, 4\n"
      "v_add_u32 v15, s70, v11\n"
      "v_mul_u32_u24 v15, 2752, v15\n"
      "s_cmp_eq_u32 s57, 21\n"
      "s_cselect_b32 s70, 4, 0\n"
      "v_and_b32 v17, s70, v14\n"
      "v_lshl_add_u32 v15, v17, 28, v15\n"
      "v_lshlrev_b32 v16, 4, v14\n"
      "s_mov_b32 s90, 20992\n"
      "v_add_u32 v7, v15, v16\n"
      "v_xor_b32 v17, 32, v16\n"
      "v_add3_u32 v8, v15, v17, s90\n"
      "s_lshl_b32 s70, s56, 2\n"
      "v_add_u32 v17, s70, v13\n"
      "v_and_b32 v17, 7, v17\n"
      "v_xor_b32 v17, v17, v12\n"
      "v_lshlrev_b32 v17, 4, v17\n"
      "s_lshl_b32 s70, s56, 3\n"
      "v_add_u32 v15, s70, v11\n"
      "v_lshl_add_u32 v6, v15, 10, v17\n"
      "v_add_u32 v10, 32768, v6\n"
      "s_lshl_b32 s36, s61, 16\n"
      "s_lshl_b32 s44, s57, 7\n"
      "s_lshl_b32 s47, s56, 10\n"
      "s_lshl_b32 s39, s56, 11\n"
      "s_and_b32 s22, s67, 0x200000\n"
      "s_mov_b32 s23, 0x20000\n"
      "s_and_b32 s26, s67, 0x158000\n"
      "s_mov_b32 s27, 0x20000\n"
      "s_and_b32 s30, s67, 0x158000\n"
      "s_mov_b32 s31, 0x20000\n"
      "s_waitcnt lgkmcnt(0)\n"
      "s_mov_b32 s24, s10\n"
      "s_and_b32 s25, s11, 0xffff\n"
      "s_mov_b32 s28, s12\n"
      "s_and_b32 s29, s13, 0xffff\n"
      "s_mov_b32 s20, s8\n"
      "s_and_b32 s21, s9, 0xffff\n"
      "s_mov_b32 s80, s45\n"
      "s_mul_i32 s81, s45, 176128\n"
      "s_add_u32 m0, s39, 65536\n"
      "s_add_u32 s81, s81, s44\n"
      "buffer_load_dwordx4 v7, s[24:27], s81 offen lds\n"
      "buffer_load_dwordx4 v8, s[24:27], s81 offen offset:1024 lds\n"
      "s_add_u32 m0, s39, 73728\n"
      "s_add_u32 s45, s45, 1\n"
      "s_and_b32 s45, s45, 7\n"
      "buffer_load_dwordx4 v7, s[28:31], s81 offen lds\n"
      "buffer_load_dwordx4 v8, s[28:31], s81 offen offset:1024 lds\n"
      "s_mul_i32 s81, s45, 176128\n"
      "s_add_u32 m0, s39, 81920\n"
      "s_add_u32 s81, s81, s44\n"
      "buffer_load_dwordx4 v7, s[24:27], s81 offen lds\n"
      "buffer_load_dwordx4 v8, s[24:27], s81 offen offset:1024 lds\n"
      "s_add_u32 m0, s39, 90112\n"
      "s_add_u32 s45, s45, 1\n"
      "s_and_b32 s45, s45, 7\n"
      "buffer_load_dwordx4 v7, s[28:31], s81 offen lds\n"
      "buffer_load_dwordx4 v8, s[28:31], s81 offen offset:1024 lds\n"
      "s_mul_i32 s81, s45, 176128\n"
      "s_add_u32 m0, s39, 98304\n"
      "s_add_u32 s81, s81, s44\n"
      "buffer_load_dwordx4 v7, s[24:27], s81 offen lds\n"
      "buffer_load_dwordx4 v8, s[24:27], s81 offen offset:1024 lds\n"
      "s_add_u32 m0, s39, 106496\n"
      "s_add_u32 s45, s45, 1\n"
      "s_and_b32 s45, s45, 7\n"
      "buffer_load_dwordx4 v7, s[28:31], s81 offen lds\n"
      "buffer_load_dwordx4 v8, s[28:31], s81 offen offset:1024 lds\n"
      "s_add_u32 s92, s18, 0x560000\n"
      "s_addc_u32 s93, s19, 0\n"
      "s_load_dwordx2 s[68:69], s[92:93], 0x1e000\n"
      "s_mul_i32 s77, s58, 3\n"
      "s_bfe_u32 s78, s59, 0x20002\n"
      "s_add_u32 s62, s77, s78\n"
      "s_bfe_u32 s78, s59, 0x20004\n"
      "s_add_u32 s63, s77, s78\n"
      "s_lshr_b32 s77, s76, 3\n"
      "s_cmp_ge_u32 s77, 11\n"
      "s_cselect_b32 s78, 1, 0\n"
      "s_cselect_b32 s66, 28, 0\n"
      "s_cmp_ge_u32 s77, 22\n"
      "s_cselect_b32 s78, 2, s78\n"
      "s_cselect_b32 s66, 14, s66\n"
      "s_mul_i32 s77, s77, 3\n"
      "s_lshl_b32 s78, s78, 5\n"
      "s_sub_u32 s64, s77, s78\n"
      "s_and_b32 s65, s76, 7\n"
      "s_mov_b32 s40, s92\n"
      "s_and_b32 s41, s93, 0xffff\n"
      "s_mov_b32 s42, 0x1e080\n"
      "s_mov_b32 s43, 0x20000\n"
      "s_mov_b32 s32, s14\n"
      "s_and_b32 s33, s15, 0xffff\n"
      "s_mov_b32 s34, 0x158000\n"
      "s_mov_b32 s35, 0x20000\n"
      "s_lshl_b32 s37, s62, 16\n"
      "s_lshl_b32 s38, s63, 16\n"
      "s_lshl_b32 s46, s66, 15\n"
      "s_and_b32 s70, s56, 1\n"
      "s_lshl_b32 s70, s70, 5\n"
      "v_xor_b32 v16, s70, v16\n"
      "v_lshl_add_u32 v16, v15, 10, v16\n"
      "s_lshl_b32 s70, s65, 7\n"
      "v_add_u32 v9, s70, v16\n"
      "s_mul_i32 s81, s45, 176128\n"
      "s_add_u32 m0, s39, 114688\n"
      "s_add_u32 s81, s81, s44\n"
      "buffer_load_dwordx4 v7, s[24:27], s81 offen lds\n"
      "buffer_load_dwordx4 v8, s[24:27], s81 offen offset:1024 lds\n"
      "s_add_u32 m0, s39, 122880\n"
      "s_add_u32 s45, s45, 1\n"
      "s_and_b32 s45, s45, 7\n"
      "buffer_load_dwordx4 v7, s[28:31], s81 offen lds\n"
      "buffer_load_dwordx4 v8, s[28:31], s81 offen offset:1024 lds\n"
      "s_mul_i32 s81, s45, 176128\n"
      "s_add_u32 m0, s39, 131072\n"
      "s_add_u32 s81, s81, s44\n"
      "buffer_load_dwordx4 v7, s[24:27], s81 offen lds\n"
      "buffer_load_dwordx4 v8, s[24:27], s81 offen offset:1024 lds\n"
      "s_add_u32 m0, s39, 139264\n"
      "s_add_u32 s45, s45, 1\n"
      "s_and_b32 s45, s45, 7\n"
      "buffer_load_dwordx4 v7, s[28:31], s81 offen lds\n"
      "buffer_load_dwordx4 v8, s[28:31], s81 offen offset:1024 lds\n"
      "s_lshl_b32 s82, s80, 7\n"
      "s_add_u32 m0, s47, 0\n"
      "s_add_u32 s82, s82, s36\n"
      "buffer_load_dwordx4 v6, s[20:23], s82 offen lds\n"
      "s_add_u32 m0, s47, 32768\n"
      "s_add_u32 s80, s80, 1\n"
      "s_and_b32 s80, s80, 7\n"
      "buffer_load_dwordx4 v10, s[20:23], s82 offen lds\n"
      "s_lshl_b32 s82, s80, 7\n"
      "s_add_u32 m0, s47, 4096\n"
      "s_add_u32 s82, s82, s36\n"
      "buffer_load_dwordx4 v6, s[20:23], s82 offen lds\n"
      "s_add_u32 m0, s47, 36864\n"
      "s_add_u32 s80, s80, 1\n"
      "s_and_b32 s80, s80, 7\n"
      "buffer_load_dwordx4 v10, s[20:23], s82 offen lds\n"
      "s_waitcnt lgkmcnt(0)\n"
      "s_mul_i32 s70, s68, 0x5851f42d\n"
      "s_mul_i32 s71, s69, 0x4c957f2d\n"
      "s_mul_hi_u32 s72, s68, 0x4c957f2d\n"
      "s_mul_i32 s73, s68, 0x4c957f2d\n"
      "s_add_u32 s70, s70, s71\n"
      "s_add_u32 s70, s70, s72\n"
      "s_add_u32 s68, s73, 0xf767814f\n"
      "s_addc_u32 s69, s70, 0x14057b7e\n"
      "s_waitcnt vmcnt(8)\n"
      "s_barrier\n"
      "s_mul_i32 s81, s45, 176128\n"
      "s_add_u32 m0, s39, 147456\n"
      "s_add_u32 s81, s81, s44\n"
      "buffer_load_dwordx4 v7, s[24:27], s81 offen lds\n"
      "buffer_load_dwordx4 v8, s[24:27], s81 offen offset:1024 lds\n"
      "s_add_u32 m0, s39, 155648\n"
      "s_add_u32 s45, s45, 1\n"
      "s_and_b32 s45, s45, 7\n"
      "buffer_load_dwordx4 v7, s[28:31], s81 offen lds\n"
      "buffer_load_dwordx4 v8, s[28:31], s81 offen offset:1024 lds\n"
      "s_lshl_b32 s82, s80, 7\n"
      "s_add_u32 m0, s47, 8192\n"
      "s_add_u32 s82, s82, s36\n"
      "buffer_load_dwordx4 v6, s[20:23], s82 offen lds\n"
      "s_add_u32 m0, s47, 40960\n"
      "s_add_u32 s80, s80, 1\n"
      "s_and_b32 s80, s80, 7\n"
      "buffer_load_dwordx4 v10, s[20:23], s82 offen lds\n"
      "s_lshl_b32 s82, s80, 7\n"
      "s_add_u32 m0, s47, 12288\n"
      "s_add_u32 s82, s82, s36\n"
      "buffer_load_dwordx4 v6, s[20:23], s82 offen lds\n"
      "s_add_u32 m0, s47, 45056\n"
      "s_add_u32 s80, s80, 1\n"
      "s_and_b32 s80, s80, 7\n"
      "buffer_load_dwordx4 v10, s[20:23], s82 offen lds\n"
      "s_waitcnt vmcnt(8)\n"
      "s_barrier\n"
      "s_lshl_b32 s70, s84, 2\n"
      "s_add_u32 s70, s70, s56\n"
      "s_mul_i32 s70, s70, 1376\n"
      "s_and_b32 s70, s70, -64\n"
      "s_add_u32 s16, s14, s70\n"
      "s_addc_u32 s17, s15, 0\n"
      "s_load_dword s83, s[16:17], 0x0\n"
      "s_load_dword s83, s[16:17], 0x40\n"
      "s_load_dword s83, s[16:17], 0x80\n"
      "s_load_dword s83, s[16:17], 0xc0\n"
      "s_load_dword s83, s[16:17], 0x100\n"
      "s_load_dword s83, s[16:17], 0x140\n"
      "s_load_dword s83, s[16:17], 0x180\n"
      "s_load_dword s83, s[16:17], 0x1c0\n"
      "s_load_dword s83, s[16:17], 0x200\n"
      "s_load_dword s83, s[16:17], 0x240\n"
      "s_load_dword s83, s[16:17], 0x280\n"
      "s_mul_i32 s81, s45, 176128\n"
      "s_add_u32 m0, s39, 65536\n"
      "s_add_u32 s81, s81, s44\n"
      "buffer_load_dwordx4 v7, s[24:27], s81 offen lds\n"
      "buffer_load_dwordx4 v8, s[24:27], s81 offen offset:1024 lds\n"
      "s_add_u32 m0, s39, 73728\n"
      "s_add_u32 s45, s45, 1\n"
      "s_and_b32 s45, s45, 7\n"
      "buffer_load_dwordx4 v7, s[28:31], s81 offen lds\n"
      "buffer_load_dwordx4 v8, s[28:31], s81 offen offset:1024 lds\n"
      "s_lshl_b32 s82, s80, 7\n"
      "s_add_u32 m0, s47, 16384\n"
      "s_add_u32 s82, s82, s36\n"
      "buffer_load_dwordx4 v6, s[20:23], s82 offen lds\n"
      "s_add_u32 m0, s47, 49152\n"
      "s_add_u32 s80, s80, 1\n"
      "s_and_b32 s80, s80, 7\n"
      "buffer_load_dwordx4 v10, s[20:23], s82 offen lds\n"
      "s_lshl_b32 s82, s80, 7\n"
      "s_add_u32 m0, s47, 20480\n"
      "s_add_u32 s82, s82, s36\n"
      "buffer_load_dwordx4 v6, s[20:23], s82 offen lds\n"
      "s_add_u32 m0, s47, 53248\n"
      "s_add_u32 s80, s80, 1\n"
      "s_and_b32 s80, s80, 7\n"
      "buffer_load_dwordx4 v10, s[20:23], s82 offen lds\n"
      "s_mul_i32 s81, s45, 176128\n"
      "s_add_u32 m0, s39, 81920\n"
      "s_add_u32 s81, s81, s44\n"
      "buffer_load_dwordx4 v7, s[24:27], s81 offen lds\n"
      "buffer_load_dwordx4 v8, s[24:27], s81 offen offset:1024 lds\n"
      "s_add_u32 m0, s39, 90112\n"
      "s_add_u32 s45, s45, 1\n"
      "s_and_b32 s45, s45, 7\n"
      "buffer_load_dwordx4 v7, s[28:31], s81 offen lds\n"
      "buffer_load_dwordx4 v8, s[28:31], s81 offen offset:1024 lds\n"
      "s_waitcnt vmcnt(12)\n"
      "s_barrier\n"
      "s_load_dword s83, s[16:17], 0x2c0\n"
      "s_load_dword s83, s[16:17], 0x300\n"
      "s_load_dword s83, s[16:17], 0x340\n"
      "s_load_dword s83, s[16:17], 0x380\n"
      "s_load_dword s83, s[16:17], 0x3c0\n"
      "s_load_dword s83, s[16:17], 0x400\n"
      "s_load_dword s83, s[16:17], 0x440\n"
      "s_load_dword s83, s[16:17], 0x480\n"
      "s_load_dword s83, s[16:17], 0x4c0\n"
      "s_load_dword s83, s[16:17], 0x500\n"
      "s_load_dword s83, s[16:17], 0x540\n"
      "s_lshl_b32 s82, s80, 7\n"
      "s_add_u32 m0, s47, 24576\n"
      "s_add_u32 s82, s82, s36\n"
      "buffer_load_dwordx4 v6, s[20:23], s82 offen lds\n"
      "s_add_u32 m0, s47, 57344\n"
      "s_add_u32 s80, s80, 1\n"
      "s_and_b32 s80, s80, 7\n"
      "buffer_load_dwordx4 v10, s[20:23], s82 offen lds\n"
      "s_lshl_b32 s82, s80, 7\n"
      "s_add_u32 m0, s47, 28672\n"
      "s_add_u32 s82, s82, s36\n"
      "buffer_load_dwordx4 v6, s[20:23], s82 offen lds\n"
      "s_add_u32 m0, s47, 61440\n"
      "s_add_u32 s80, s80, 1\n"
      "s_and_b32 s80, s80, 7\n"
      "buffer_load_dwordx4 v10, s[20:23], s82 offen lds\n"
      "s_lshl_b32 s82, s80, 7\n"
      "s_add_u32 m0, s47, 98304\n"
      "s_add_u32 s82, s82, s37\n"
      "buffer_load_dwordx4 v6, s[20:23], s82 offen lds\n"
      "s_add_u32 s80, s80, 1\n"
      "s_and_b32 s80, s80, 7\n"
      "s_lshl_b32 s82, s80, 7\n"
      "s_add_u32 m0, s47, 102400\n"
      "s_add_u32 s82, s82, s37\n"
      "buffer_load_dwordx4 v6, s[20:23], s82 offen lds\n"
      "s_add_u32 s80, s80, 1\n"
      "s_and_b32 s80, s80, 7\n"
      "s_lshl_b32 s82, s80, 7\n"
      "s_add_u32 m0, s47, 106496\n"
      "s_add_u32 s82, s82, s37\n"
      "buffer_load_dwordx4 v6, s[20:23], s82 offen lds\n"
      "s_add_u32 s80, s80, 1\n"
      "s_and_b32 s80, s80, 7\n"
      "s_lshl_b32 s82, s80, 7\n"
      "s_add_u32 m0, s47, 110592\n"
      "s_add_u32 s82, s82, s37\n"
      "buffer_load_dwordx4 v6, s[20:23], s82 offen lds\n"
      "s_add_u32 s80, s80, 1\n"
      "s_and_b32 s80, s80, 7\n"
      "s_waitcnt vmcnt(12)\n"
      "s_barrier\n"
      "s_lshl_b32 s82, s80, 7\n"
      "s_add_u32 m0, s47, 114688\n"
      "s_add_u32 s82, s82, s37\n"
      "buffer_load_dwordx4 v6, s[20:23], s82 offen lds\n"
      "s_add_u32 s80, s80, 1\n"
      "s_and_b32 s80, s80, 7\n"
      "s_lshl_b32 s82, s80, 7\n"
      "s_add_u32 m0, s47, 118784\n"
      "s_add_u32 s82, s82, s37\n"
      "buffer_load_dwordx4 v6, s[20:23], s82 offen lds\n"
      "s_add_u32 s80, s80, 1\n"
      "s_and_b32 s80, s80, 7\n"
      "s_lshl_b32 s82, s80, 7\n"
      "s_add_u32 m0, s47, 122880\n"
      "s_add_u32 s82, s82, s37\n"
      "buffer_load_dwordx4 v6, s[20:23], s82 offen lds\n"
      "s_add_u32 s80, s80, 1\n"
      "s_and_b32 s80, s80, 7\n"
      "s_lshl_b32 s82, s80, 7\n"
      "s_add_u32 m0, s47, 126976\n"
      "s_add_u32 s82, s82, s37\n"
      "buffer_load_dwordx4 v6, s[20:23], s82 offen lds\n"
      "s_add_u32 s80, s80, 1\n"
      "s_and_b32 s80, s80, 7\n"
      "s_lshl_b32 s82, s80, 7\n"
      "s_add_u32 m0, s47, 131072\n"
      "s_add_u32 s82, s82, s37\n"
      "buffer_load_dwordx4 v10, s[20:23], s82 offen lds\n"
      "s_add_u32 s80, s80, 1\n"
      "s_and_b32 s80, s80, 7\n"
      "s_lshl_b32 s82, s80, 7\n"
      "s_add_u32 m0, s47, 135168\n"
      "s_add_u32 s82, s82, s37\n"
      "buffer_load_dwordx4 v10, s[20:23], s82 offen lds\n"
      "s_add_u32 s80, s80, 1\n"
      "s_and_b32 s80, s80, 7\n"
      "s_lshl_b32 s82, s80, 7\n"
      "s_add_u32 m0, s47, 139264\n"
      "s_add_u32 s82, s82, s37\n"
      "buffer_load_dwordx4 v10, s[20:23], s82 offen lds\n"
      "s_add_u32 s80, s80, 1\n"
      "s_and_b32 s80, s80, 7\n"
      "s_lshl_b32 s82, s80, 7\n"
      "s_add_u32 m0, s47, 143360\n"
      "s_add_u32 s82, s82, s37\n"
      "buffer_load_dwordx4 v10, s[20:23], s82 offen lds\n"
      "s_add_u32 s80, s80, 1\n"
      "s_and_b32 s80, s80, 7\n"
      "s_waitcnt vmcnt(12)\n"
      "s_barrier\n"
      "s_lshl_b32 s82, s80, 7\n"
      "s_add_u32 m0, s47, 147456\n"
      "s_add_u32 s82, s82, s37\n"
      "buffer_load_dwordx4 v10, s[20:23], s82 offen lds\n"
      "s_add_u32 s80, s80, 1\n"
      "s_and_b32 s80, s80, 7\n"
      "s_lshl_b32 s82, s80, 7\n"
      "s_add_u32 m0, s47, 151552\n"
      "s_add_u32 s82, s82, s37\n"
      "buffer_load_dwordx4 v10, s[20:23], s82 offen lds\n"
      "s_add_u32 s80, s80, 1\n"
      "s_and_b32 s80, s80, 7\n"
      "s_lshl_b32 s82, s80, 7\n"
      "s_add_u32 m0, s47, 155648\n"
      "s_add_u32 s82, s82, s37\n"
      "buffer_load_dwordx4 v10, s[20:23], s82 offen lds\n"
      "s_add_u32 s80, s80, 1\n"
      "s_and_b32 s80, s80, 7\n"
      "s_lshl_b32 s82, s80, 7\n"
      "s_add_u32 m0, s47, 159744\n"
      "s_add_u32 s82, s82, s37\n"
      "buffer_load_dwordx4 v10, s[20:23], s82 offen lds\n"
      "s_add_u32 s80, s80, 1\n"
      "s_and_b32 s80, s80, 7\n"
      "s_waitcnt vmcnt(12)\n"
      "s_barrier\n"
      "s_mul_i32 s70, s64, 2816\n"
      "s_add_u32 s92, s92, s70\n"
      "s_addc_u32 s93, s93, 0\n"
      "s_lshr_b32 s71, s66, 1\n"
      "s_lshl_b32 s72, s56, 1\n"
      "s_add_u32 s73, s71, s72\n"
      "s_mul_i32 s8, s73, 128\n"
      "s_cmp_eq_u32 s71, 14\n"
      "s_cselect_b32 s70, 7, 6\n"
      "s_add_u32 s73, s72, 1\n"
      "s_min_u32 s73, s73, s70\n"
      "s_add_u32 s73, s73, s71\n"
      "s_mul_i32 s9, s73, 128\n"
      "s_add_u32 s71, s71, 7\n"
      "s_cmp_eq_u32 s71, 21\n"
      "s_cselect_b32 s71, 0, s71\n"
      "s_add_u32 s73, s71, s72\n"
      "s_mul_i32 s10, s73, 128\n"
      "s_cmp_eq_u32 s71, 14\n"
      "s_cselect_b32 s70, 7, 6\n"
      "s_add_u32 s73, s72, 1\n"
      "s_min_u32 s73, s73, s70\n"
      "s_add_u32 s73, s73, s71\n"
      "s_mul_i32 s11, s73, 128\n"
      "s_add_u32 s71, s71, 7\n"
      "s_cmp_eq_u32 s71, 21\n"
      "s_cselect_b32 s71, 0, s71\n"
      "s_add_u32 s73, s71, s72\n"
      "s_mul_i32 s12, s73, 128\n"
      "s_cmp_eq_u32 s71, 14\n"
      "s_cselect_b32 s70, 7, 6\n"
      "s_add_u32 s73, s72, 1\n"
      "s_min_u32 s73, s73, s70\n"
      "s_add_u32 s73, s73, s71\n"
      "s_mul_i32 s13, s73, 128\n"
      "s_add_u32 m0, s47, 0\n"
      "s_nop 0\n"
      "buffer_load_dwordx4 v9, s[32:35], s46 offen lds\n"
      "s_add_u32 s46, s46, 32768\n"
      "s_add_u32 m0, s47, 4096\n"
      "s_nop 0\n"
      "buffer_load_dwordx4 v9, s[32:35], s46 offen lds\n"
      "s_add_u32 s46, s46, 32768\n"
      "s_add_u32 m0, s47, 8192\n"
      "s_nop 0\n"
      "buffer_load_dwordx4 v9, s[32:35], s46 offen lds\n"
      "s_add_u32 s46, s46, 32768\n"
      "s_add_u32 m0, s47, 12288\n"
      "s_nop 0\n"
      "buffer_load_dwordx4 v9, s[32:35], s46 offen lds\n"
      "s_add_u32 s46, s46, 32768\n"
      "s_waitcnt vmcnt(12)\n"
      "s_barrier\n"
      "s_lshl_b32 s82, s80, 7\n"
      "s_add_u32 m0, s47, 98304\n"
      "s_add_u32 s82, s82, s38\n"
      "buffer_load_dwordx4 v6, s[20:23], s82 offen lds\n"
      "s_add_u32 s80, s80, 1\n"
      "s_and_b32 s80, s80, 7\n"
      "s_add_u32 m0, s47, 16384\n"
      "s_nop 0\n"
      "buffer_load_dwordx4 v9, s[32:35], s46 offen lds\n"
      "s_add_u32 s46, s46, 32768\n"
      "s_lshl_b32 s82, s80, 7\n"
      "s_add_u32 m0, s47, 102400\n"
      "s_add_u32 s82, s82, s38\n"
      "buffer_load_dwordx4 v6, s[20:23], s82 offen lds\n"
      "s_add_u32 s80, s80, 1\n"
      "s_and_b32 s80, s80, 7\n"
      "s_add_u32 m0, s47, 20480\n"
      "s_nop 0\n"
      "buffer_load_dwordx4 v9, s[32:35], s46 offen lds\n"
      "s_add_u32 s46, s46, 32768\n"
      "s_lshl_b32 s82, s80, 7\n"
      "s_add_u32 m0, s47, 106496\n"
      "s_add_u32 s82, s82, s38\n"
      "buffer_load_dwordx4 v6, s[20:23], s82 offen lds\n"
      "s_add_u32 s80, s80, 1\n"
      "s_and_b32 s80, s80, 7\n"
      "s_lshl_b32 s82, s80, 7\n"
      "s_add_u32 m0, s47, 110592\n"
      "s_add_u32 s82, s82, s38\n"
      "buffer_load_dwordx4 v6, s[20:23], s82 offen lds\n"
      "s_add_u32 s80, s80, 1\n"
      "s_and_b32 s80, s80, 7\n"
      "s_waitcnt vmcnt(14)\n"
      "s_barrier\n"
      "s_lshl_b32 s82, s80, 7\n"
      "s_add_u32 m0, s47, 114688\n"
      "s_add_u32 s82, s82, s38\n"
      "buffer_load_dwordx4 v6, s[20:23], s82 offen lds\n"
      "s_add_u32 s80, s80, 1\n"
      "s_and_b32 s80, s80, 7\n"
      "s_add_u32 m0, s47, 24576\n"
      "s_nop 0\n"
      "buffer_load_dwordx4 v9, s[32:35], s46 offen lds\n"
      "s_add_u32 s46, s46, 32768\n"
      "s_lshl_b32 s82, s80, 7\n"
      "s_add_u32 m0, s47, 118784\n"
      "s_add_u32 s82, s82, s38\n"
      "buffer_load_dwordx4 v6, s[20:23], s82 offen lds\n"
      "s_add_u32 s80, s80, 1\n"
      "s_and_b32 s80, s80, 7\n"
      "s_lshl_b32 s82, s80, 7\n"
      "s_add_u32 m0, s47, 122880\n"
      "s_add_u32 s82, s82, s38\n"
      "buffer_load_dwordx4 v6, s[20:23], s82 offen lds\n"
      "s_add_u32 s80, s80, 1\n"
      "s_and_b32 s80, s80, 7\n"
      "s_lshl_b32 s82, s80, 7\n"
      "s_add_u32 m0, s47, 126976\n"
      "s_add_u32 s82, s82, s38\n"
      "buffer_load_dwordx4 v6, s[20:23], s82 offen lds\n"
      "s_add_u32 s80, s80, 1\n"
      "s_and_b32 s80, s80, 7\n"
      "s_waitcnt vmcnt(15)\n"
      "s_barrier\n"
      "s_lshl_b32 s82, s80, 7\n"
      "s_add_u32 m0, s47, 131072\n"
      "s_add_u32 s82, s82, s38\n"
      "buffer_load_dwordx4 v10, s[20:23], s82 offen lds\n"
      "s_add_u32 s80, s80, 1\n"
      "s_and_b32 s80, s80, 7\n"
      "s_add_u32 m0, s47, 28672\n"
      "s_nop 0\n"
      "buffer_load_dwordx4 v9, s[32:35], s46 offen lds\n"
      "s_add_u32 s46, s46, 32768\n"
      "s_lshl_b32 s82, s80, 7\n"
      "s_add_u32 m0, s47, 135168\n"
      "s_add_u32 s82, s82, s38\n"
      "buffer_load_dwordx4 v10, s[20:23], s82 offen lds\n"
      "s_add_u32 s80, s80, 1\n"
      "s_and_b32 s80, s80, 7\n"
      "s_add_u32 m0, s47, 32768\n"
      "s_nop 0\n"
      "buffer_load_dwordx4 v9, s[32:35], s46 offen lds\n"
      "s_add_u32 s46, s46, 32768\n"
      "s_lshl_b32 s82, s80, 7\n"
      "s_add_u32 m0, s47, 139264\n"
      "s_add_u32 s82, s82, s38\n"
      "buffer_load_dwordx4 v10, s[20:23], s82 offen lds\n"
      "s_add_u32 s80, s80, 1\n"
      "s_and_b32 s80, s80, 7\n"
      "s_lshl_b32 s82, s80, 7\n"
      "s_add_u32 m0, s47, 143360\n"
      "s_add_u32 s82, s82, s38\n"
      "buffer_load_dwordx4 v10, s[20:23], s82 offen lds\n"
      "s_add_u32 s80, s80, 1\n"
      "s_and_b32 s80, s80, 7\n"
      "s_waitcnt vmcnt(11)\n"
      "s_barrier\n"
      "s_lshl_b32 s82, s80, 7\n"
      "s_add_u32 m0, s47, 147456\n"
      "s_add_u32 s82, s82, s38\n"
      "buffer_load_dwordx4 v10, s[20:23], s82 offen lds\n"
      "s_add_u32 s80, s80, 1\n"
      "s_and_b32 s80, s80, 7\n"
      "s_add_u32 m0, s47, 36864\n"
      "s_nop 0\n"
      "buffer_load_dwordx4 v9, s[32:35], s46 offen lds\n"
      "s_add_u32 s46, s46, 32768\n"
      "s_lshl_b32 s82, s80, 7\n"
      "s_add_u32 m0, s47, 151552\n"
      "s_add_u32 s82, s82, s38\n"
      "buffer_load_dwordx4 v10, s[20:23], s82 offen lds\n"
      "s_add_u32 s80, s80, 1\n"
      "s_and_b32 s80, s80, 7\n"
      "s_lshl_b32 s82, s80, 7\n"
      "s_add_u32 m0, s47, 155648\n"
      "s_add_u32 s82, s82, s38\n"
      "buffer_load_dwordx4 v10, s[20:23], s82 offen lds\n"
      "s_add_u32 s80, s80, 1\n"
      "s_and_b32 s80, s80, 7\n"
      "s_lshl_b32 s82, s80, 7\n"
      "s_add_u32 m0, s47, 159744\n"
      "s_add_u32 s82, s82, s38\n"
      "buffer_load_dwordx4 v10, s[20:23], s82 offen lds\n"
      "s_add_u32 s80, s80, 1\n"
      "s_and_b32 s80, s80, 7\n"
      "s_waitcnt vmcnt(11)\n"
      "s_barrier\n"
      "s_lshl_b32 s70, s84, 3\n"
      "s_add_u32 s70, s70, s56\n"
      "s_cmp_lg_u32 s70, 1984\n"
      "s_cbranch_scc1 .Lglu_L_NOBUMP\n"
      "v_lshlrev_b32 v11, 7, v2\n"
      "s_mov_b32 s72, 20000\n"
      ".Lglu_L_BUMP:\n"
      "s_mov_b32 s70, 0x16000\n"
      "buffer_load_dwordx2 v[12:13], v11, s[40:43], s70 offen sc1\n"
      "s_add_u32 s70, s70, 8192\n"
      "buffer_load_dwordx2 v[14:15], v11, s[40:43], s70 offen sc1\n"
      "s_add_u32 s70, s70, 8192\n"
      "buffer_load_dwordx2 v[16:17], v11, s[40:43], s70 offen sc1\n"
      "s_add_u32 s70, s70, 8192\n"
      "buffer_load_dwordx2 v[18:19], v11, s[40:43], s70 offen sc1\n"
      "s_add_u32 s70, s70, 8192\n"
      "s_waitcnt vmcnt(0)\n"
      "v_cmp_eq_u64 s[70:71], s[68:69], v[12:13]\n"
      "v_cmp_eq_u64 vcc, s[68:69], v[14:15]\n"
      "s_and_b64 s[70:71], s[70:71], vcc\n"
      "v_cmp_eq_u64 vcc, s[68:69], v[16:17]\n"
      "s_and_b64 s[70:71], s[70:71], vcc\n"
      "v_cmp_eq_u64 vcc, s[68:69], v[18:19]\n"
      "s_and_b64 s[70:71], s[70:71], vcc\n"
      "s_cmp_eq_u64 s[70:71], exec\n"
      "s_cbranch_scc1 .Lglu_L_BUMPOK\n"
      "s_sleep 8\n"
      "s_sub_u32 s72, s72, 1\n"
      "s_cmp_lg_u32 s72, 0\n"
      "s_cbranch_scc1 .Lglu_L_BUMP\n"
      ".Lglu_L_BUMPOK:\n"
      "v_mov_b32 v12, s68\n"
      "v_mov_b32 v13, s69\n"
      "v_lshlrev_b32 v11, 20, v2\n"
      "s_mov_b32 s70, 0x1e000\n"
      "buffer_store_dwordx2 v[12:13], v11, s[40:43], s70 offen sc1\n"
      "s_waitcnt vmcnt(0)\n"
      ".Lglu_L_NOBUMP:\n"
      "s_add_u32 m0, s47, 40960\n"
      "s_nop 0\n"
      "buffer_load_dwordx4 v9, s[32:35], s46 offen lds\n"
      "s_add_u32 s46, s46, 32768\n"
      "s_add_u32 m0, s47, 45056\n"
      "s_nop 0\n"
      "buffer_load_dwordx4 v9, s[32:35], s46 offen lds\n"
      "s_add_u32 s46, s46, 32768\n"
      "s_add_u32 m0, s47, 49152\n"
      "s_nop 0\n"
      "buffer_load_dwordx4 v9, s[32:35], s46 offen lds\n"
      "s_add_u32 s46, s46, 32768\n"
      "s_add_u32 m0, s47, 53248\n"
      "s_nop 0\n"
      "buffer_load_dwordx4 v9, s[32:35], s46 offen lds\n"
      "s_add_u32 s46, s46, 32768\n"
      "s_add_u32 m0, s47, 57344\n"
      "s_nop 0\n"
      "buffer_load_dwordx4 v9, s[32:35], s46 offen lds\n"
      "s_add_u32 s46, s46, 32768\n"
      "s_waitcnt vmcnt(10)\n"
      "s_barrier\n"
      "s_add_u32 m0, s47, 61440\n"
      "s_cmp_lg_u32 s46, 1409024\n"
      "s_cselect_b32 s46, s46, 0\n"
      "buffer_load_dwordx4 v9, s[32:35], s46 offen lds\n"
      "s_add_u32 s46, s46, 32768\n"
      "s_add_u32 m0, s47, 65536\n"
      "s_nop 0\n"
      "buffer_load_dwordx4 v9, s[32:35], s46 offen lds\n"
      "s_add_u32 s46, s46, 32768\n"
      "s_add_u32 m0, s47, 69632\n"
      "s_nop 0\n"
      "buffer_load_dwordx4 v9, s[32:35], s46 offen lds\n"
      "s_add_u32 s46, s46, 32768\n"
      "s_add_u32 m0, s47, 73728\n"
      "s_nop 0\n"
      "buffer_load_dwordx4 v9, s[32:35], s46 offen lds\n"
      "s_add_u32 s46, s46, 32768\n"
      "s_add_u32 m0, s47, 77824\n"
      "s_nop 0\n"
      "buffer_load_dwordx4 v9, s[32:35], s46 offen lds\n"
      "s_add_u32 s46, s46, 32768\n"
      "s_waitcnt vmcnt(10)\n"
      "s_barrier\n"
      "s_load_dwordx8 s[24:31], s[92:93], s8 glc\n"
      "s_load_dwordx8 s[48:55], s[92:93], s9 glc\n"
      "s_add_u32 m0, s47, 81920\n"
      "s_nop 0\n"
      "buffer_load_dwordx4 v9, s[32:35], s46 offen lds\n"
      "s_add_u32 s46, s46, 32768\n"
      "s_add_u32 m0, s47, 86016\n"
      "s_nop 0\n"
      "buffer_load_dwordx4 v9, s[32:35], s46 offen lds\n"
      "s_add_u32 s46, s46, 32768\n"
      "s_add_u32 m0, s47, 90112\n"
      "s_nop 0\n"
      "buffer_load_dwordx4 v9, s[32:35], s46 offen lds\n"
      "s_add_u32 s46, s46, 32768\n"
      "s_add_u32 m0, s47, 94208\n"
      "s_nop 0\n"
      "buffer_load_dwordx4 v9, s[32:35], s46 offen lds\n"
      "s_add_u32 s46, s46, 32768\n"
      "s_waitcnt vmcnt(6)\n"
      "s_mov_b32 s72, 20000\n"
      ".Lglu_L_P0:\n"
      "s_waitcnt lgkmcnt(0)\n"
      "s_xor_b64 s[24:25], s[24:25], s[68:69]\n"
      "s_xor_b64 s[26:27], s[26:27], s[68:69]\n"
      "s_or_b64 s[24:25], s[24:25], s[26:27]\n"
      "s_xor_b64 s[28:29], s[28:29], s[68:69]\n"
      "s_or_b64 s[24:25], s[24:25], s[28:29]\n"
      "s_xor_b64 s[30:31], s[30:31], s[68:69]\n"
      "s_or_b64 s[24:25], s[24:25], s[30:31]\n"
      "s_xor_b64 s[48:49], s[48:49], s[68:69]\n"
      "s_or_b64 s[24:25], s[24:25], s[48:49]\n"
      "s_xor_b64 s[50:51], s[50:51], s[68:69]\n"
      "s_or_b64 s[24:25], s[24:25], s[50:51]\n"
      "s_xor_b64 s[52:53], s[52:53], s[68:69]\n"
      "s_or_b64 s[24:25], s[24:25], s[52:53]\n"
      "s_xor_b64 s[54:55], s[54:55], s[68:69]\n"
      "s_or_b64 s[24:25], s[24:25], s[54:55]\n"
      "s_cbranch_scc0 .Lglu_L_P0_OK\n"
      "s_load_dwordx8 s[24:31], s[92:93], s8 glc\n"
      "s_load_dwordx8 s[48:55], s[92:93], s9 glc\n"
      "s_sub_u32 s72, s72, 1\n"
      "s_cmp_lg_u32 s72, 0\n"
      "s_cbranch_scc1 .Lglu_L_P0\n"
      "s_waitcnt lgkmcnt(0)\n"
      ".Lglu_L_P0_OK:\n"
      "s_barrier\n"
      "s_add_u32 m0, s47, 98304\n"
      "s_nop 0\n"
      "buffer_load_dwordx4 v9, s[32:35], s46 offen lds\n"
      "s_add_u32 s46, s46, 32768\n"
      "s_add_u32 m0, s47, 102400\n"
      "s_nop 0\n"
      "buffer_load_dwordx4 v9, s[32:35], s46 offen lds\n"
      "s_add_u32 s46, s46, 32768\n"
      "s_add_u32 m0, s47, 106496\n"
      "s_nop 0\n"
      "buffer_load_dwordx4 v9, s[32:35], s46 offen lds\n"
      "s_add_u32 s46, s46, 32768\n"
      "s_add_u32 m0, s47, 110592\n"
      "s_nop 0\n"
      "buffer_load_dwordx4 v9, s[32:35], s46 offen lds\n"
      "s_add_u32 s46, s46, 32768\n"
      "s_add_u32 m0, s47, 114688\n"
      "s_nop 0\n"
      "buffer_load_dwordx4 v9, s[32:35], s46 offen lds\n"
      "s_add_u32 s46, s46, 32768\n"
      "s_add_u32 m0, s47, 118784\n"
      "s_cmp_lg_u32 s46, 1409024\n"
      "s_cselect_b32 s46, s46, 0\n"
      "buffer_load_dwordx4 v9, s[32:35], s46 offen lds\n"
      "s_add_u32 s46, s46, 32768\n"
      "s_load_dwordx8 s[24:31], s[92:93], s10 glc\n"
      "s_load_dwordx8 s[48:55], s[92:93], s11 glc\n"
      "s_waitcnt vmcnt(0)\n"
      "s_mov_b32 s72, 20000\n"
      ".Lglu_L_P1:\n"
      "s_waitcnt lgkmcnt(0)\n"
      "s_xor_b64 s[24:25], s[24:25], s[68:69]\n"
      "s_xor_b64 s[26:27], s[26:27], s[68:69]\n"
      "s_or_b64 s[24:25], s[24:25], s[26:27]\n"
      "s_xor_b64 s[28:29], s[28:29], s[68:69]\n"
      "s_or_b64 s[24:25], s[24:25], s[28:29]\n"
      "s_xor_b64 s[30:31], s[30:31], s[68:69]\n"
      "s_or_b64 s[24:25], s[24:25], s[30:31]\n"
      "s_xor_b64 s[48:49], s[48:49], s[68:69]\n"
      "s_or_b64 s[24:25], s[24:25], s[48:49]\n"
      "s_xor_b64 s[50:51], s[50:51], s[68:69]\n"
      "s_or_b64 s[24:25], s[24:25], s[50:51]\n"
      "s_xor_b64 s[52:53], s[52:53], s[68:69]\n"
      "s_or_b64 s[24:25], s[24:25], s[52:53]\n"
      "s_xor_b64 s[54:55], s[54:55], s[68:69]\n"
      "s_or_b64 s[24:25], s[24:25], s[54:55]\n"
      "s_cbranch_scc0 .Lglu_L_P1_OK\n"
      "s_load_dwordx8 s[24:31], s[92:93], s10 glc\n"
      "s_load_dwordx8 s[48:55], s[92:93], s11 glc\n"
      "s_sub_u32 s72, s72, 1\n"
      "s_cmp_lg_u32 s72, 0\n"
      "s_cbranch_scc1 .Lglu_L_P1\n"
      "s_waitcnt lgkmcnt(0)\n"
      ".Lglu_L_P1_OK:\n"
      "s_barrier\n"
      "s_sleep 6\n"
      "s_load_dwordx8 s[24:31], s[92:93], s12 glc\n"
      "s_load_dwordx8 s[48:55], s[92:93], s13 glc\n"
      "s_mov_b32 s72, 20000\n"
      ".Lglu_L_P2:\n"
      "s_waitcnt lgkmcnt(0)\n"
      "s_xor_b64 s[24:25], s[24:25], s[68:69]\n"
      "s_xor_b64 s[26:27], s[26:27], s[68:69]\n"
      "s_or_b64 s[24:25], s[24:25], s[26:27]\n"
      "s_xor_b64 s[28:29], s[28:29], s[68:69]\n"
      "s_or_b64 s[24:25], s[24:25], s[28:29]\n"
      "s_xor_b64 s[30:31], s[30:31], s[68:69]\n"
      "s_or_b64 s[24:25], s[24:25], s[30:31]\n"
      "s_xor_b64 s[48:49], s[48:49], s[68:69]\n"
      "s_or_b64 s[24:25], s[24:25], s[48:49]\n"
      "s_xor_b64 s[50:51], s[50:51], s[68:69]\n"
      "s_or_b64 s[24:25], s[24:25], s[50:51]\n"
      "s_xor_b64 s[52:53], s[52:53], s[68:69]\n"
      "s_or_b64 s[24:25], s[24:25], s[52:53]\n"
      "s_xor_b64 s[54:55], s[54:55], s[68:69]\n"
      "s_or_b64 s[24:25], s[24:25], s[54:55]\n"
      "s_cbranch_scc0 .Lglu_L_P2_OK\n"
      "s_load_dwordx8 s[24:31], s[92:93], s12 glc\n"
      "s_load_dwordx8 s[48:55], s[92:93], s13 glc\n"
      "s_sub_u32 s72, s72, 1\n"
      "s_cmp_lg_u32 s72, 0\n"
      "s_cbranch_scc1 .Lglu_L_P2\n"
      "s_waitcnt lgkmcnt(0)\n"
      ".Lglu_L_P2_OK:\n"
      "s_barrier\n"
      "s_endpgm\n"
      ".Lglu_C_ENTRY:\n"
      "s_setprio 2\n"
      "s_mul_i32 s77, s58, 3\n"
      "s_bfe_u32 s78, s59, 0x20002\n"
      "s_add_u32 s62, s77, s78\n"
      "s_bfe_u32 s78, s59, 0x20004\n"
      "s_add_u32 s63, s77, s78\n"
      "s_lshr_b32 s77, s76, 3\n"
      "s_cmp_ge_u32 s77, 11\n"
      "s_cselect_b32 s78, 1, 0\n"
      "s_cselect_b32 s66, 28, 0\n"
      "s_cmp_ge_u32 s77, 22\n"
      "s_cselect_b32 s78, 2, s78\n"
      "s_cselect_b32 s66, 14, s66\n"
      "s_mul_i32 s77, s77, 3\n"
      "s_lshl_b32 s78, s78, 5\n"
      "s_sub_u32 s64, s77, s78\n"
      "s_and_b32 s65, s76, 7\n"
      "s_sub_u32 s74, s56, 4\n"
      "v_and_b32 v40, 15, v2\n"
      "v_lshrrev_b32 v41, 4, v2\n"
      "v_bfe_u32 v42, v2, 1, 3\n"
      "v_xor_b32 v42, v42, v41\n"
      "v_lshlrev_b32 v42, 4, v42\n"
      "v_lshl_add_u32 v4, v40, 7, v42\n"
      "v_xor_b32 v5, 64, v4\n"
      "v_and_b32 v42, 1, v41\n"
      "v_bfe_u32 v43, v2, 2, 2\n"
      "v_and_b32 v44, 3, v2\n"
      "v_and_b32 v45, 2, v43\n"
      "v_or_b32 v45, v45, v42\n"
      "v_lshlrev_b32 v45, 5, v45\n"
      "v_lshl_add_u32 v45, v44, 3, v45\n"
      "v_lshl_add_u32 v46, v41, 3, v43\n"
      "v_lshl_add_u32 v24, v46, 7, v45\n"
      "v_xor_b32 v25, 32, v24\n"
      "v_xor_b32 v26, 64, v24\n"
      "v_xor_b32 v27, 96, v24\n"
      "s_lshl_b32 s70, s74, 5\n"
      "v_xor_b32 v45, s70, v24\n"
      "v_add_u32 v6, 65536, v45\n"
      "v_add_u32 v7, 131072, v45\n"
      "v_mul_u32_u24 v45, 44032, v42\n"
      "v_lshrrev_b32 v46, 1, v41\n"
      "v_lshl_add_u32 v45, v46, 8, v45\n"
      "v_lshl_add_u32 v8, v40, 4, v45\n"
      "v_add_u32 v9, 88064, v8\n"
      "v_lshlrev_b32 v45, 4, v2\n"
      "s_and_b32 s71, s74, 1\n"
      "s_mul_i32 s72, s71, 44032\n"
      "v_add_u32 v10, s72, v45\n"
      "s_sub_u32 s72, 44032, s72\n"
      "v_add_u32 v30, s72, v45\n"
      "s_lshl_b32 s72, s74, 12\n"
      "v_add_u32 v31, s72, v45\n"
      "s_lshl_b32 s22, s71, 12\n"
      "v_add_u32 v24, s22, v24\n"
      "v_add_u32 v25, s22, v25\n"
      "v_add_u32 v26, s22, v26\n"
      "v_add_u32 v27, s22, v27\n"
      "v_lshlrev_b32 v11, 20, v2\n"
      "v_lshrrev_b32 v45, 3, v2\n"
      "v_and_b32 v47, 7, v2\n"
      "s_lshl_b32 s71, s74, 3\n"
      "v_add_u32 v48, s71, v45\n"
      "v_lshlrev_b32 v49, 2, v42\n"
      "v_xor_b32 v49, v49, v47\n"
      "v_lshlrev_b32 v49, 4, v49\n"
      "s_and_b32 s71, s74, 1\n"
      "s_lshl_b32 s71, s71, 5\n"
      "v_xor_b32 v49, s71, v49\n"
      "v_lshl_add_u32 v48, v48, 10, v49\n"
      "s_lshl_b32 s71, s65, 7\n"
      "v_add_u32 v14, s71, v48\n"
      "s_lshl_b32 s71, s74, 11\n"
      "s_add_u32 s71, s71, 28672\n"
      "v_and_b32 v48, 7, v40\n"
      "v_xor_b32 v48, v48, v46\n"
      "v_lshlrev_b32 v48, 4, v48\n"
      "v_lshl_add_u32 v48, v42, 3, v48\n"
      "v_lshl_add_u32 v48, v40, 7, v48\n"
      "v_add_u32 v28, s71, v48\n"
      "v_xor_b32 v48, v45, v47\n"
      "v_lshlrev_b32 v48, 4, v48\n"
      "v_lshl_add_u32 v48, v45, 7, v48\n"
      "v_add_u32 v29, s71, v48\n"
      "v_lshlrev_b32 v48, 4, v47\n"
      "v_lshl_add_u32 v15, v45, 10, v48\n"
      "s_add_u32 s71, s66, 30\n"
      "s_sub_u32 s72, s71, 43\n"
      "s_min_u32 s71, s71, s72\n"
      "s_lshl_b32 s20, s71, 15\n"
      "s_lshl_b32 s21, s74, 10\n"
      "s_lshl_b32 s73, s64, 2\n"
      "s_add_u32 s73, s73, s74\n"
      "s_lshl_b32 s91, s73, 14\n"
      "s_lshl_b32 s71, s65, 7\n"
      "s_add_u32 s91, s91, s71\n"
      "s_add_u32 s90, s91, 8192\n"
      "s_and_b32 s73, s73, -2\n"
      "s_mul_i32 s73, s73, 43\n"
      "s_and_b32 s71, s74, 1\n"
      "s_add_u32 s70, s66, 0\n"
      "s_add_u32 s70, s70, s71\n"
      "s_sub_u32 s72, s70, 43\n"
      "s_min_u32 s70, s70, s72\n"
      "s_add_u32 s70, s70, s73\n"
      "s_lshl_b32 s81, s70, 10\n"
      "s_add_u32 s70, s66, 14\n"
      "s_add_u32 s70, s70, s71\n"
      "s_sub_u32 s72, s70, 43\n"
      "s_min_u32 s70, s70, s72\n"
      "s_add_u32 s70, s70, s73\n"
      "s_lshl_b32 s82, s70, 10\n"
      "s_add_u32 s70, s66, 16\n"
      "s_add_u32 s70, s70, s71\n"
      "s_sub_u32 s72, s70, 43\n"
      "s_min_u32 s70, s70, s72\n"
      "s_add_u32 s70, s70, s73\n"
      "s_lshl_b32 s83, s70, 10\n"
      "s_add_u32 s70, s66, 28\n"
      "s_sub_u32 s72, s70, 43\n"
      "s_min_u32 s70, s70, s72\n"
      "s_add_u32 s70, s70, s73\n"
      "s_lshl_b32 s23, s70, 10\n"
      "s_add_u32 s70, s66, 29\n"
      "s_sub_u32 s72, s70, 43\n"
      "s_min_u32 s70, s70, s72\n"
      "s_add_u32 s70, s70, s73\n"
      "s_lshl_b32 s27, s70, 10\n"
      "s_lshl_b32 s70, s57, 2\n"
      "s_add_u32 s70, s70, s74\n"
      "s_lshl_b32 s73, s70, 9\n"
      "s_mul_i32 s72, s57, 128\n"
      "s_lshl_b32 s71, s74, 3\n"
      "s_add_u32 s72, s72, s71\n"
      "s_mul_i32 s71, s61, 176128\n"
      "s_add_u32 s24, s71, s73\n"
      "s_mul_i32 s71, s61, 2816\n"
      "s_add_u32 s28, s71, s72\n"
      "s_mul_i32 s71, s62, 176128\n"
      "s_add_u32 s25, s71, s73\n"
      "s_mul_i32 s71, s62, 2816\n"
      "s_add_u32 s29, s71, s72\n"
      "s_mul_i32 s71, s63, 176128\n"
      "s_add_u32 s26, s71, s73\n"
      "s_mul_i32 s71, s63, 2816\n"
      "s_add_u32 s30, s71, s72\n"
      "s_cmp_lt_u32 s70, 86\n"
      "s_cselect_b32 s75, s67, 0\n"
      "s_mov_b32 s85, 0xbfb8aa3b\n"
      "s_waitcnt lgkmcnt(0)\n"
      "s_add_u32 s92, s18, 0x560000\n"
      "s_addc_u32 s93, s19, 0\n"
      "s_load_dwordx2 s[68:69], s[92:93], 0x1e000\n"
      "s_mov_b32 s32, s14\n"
      "s_and_b32 s33, s15, 0xffff\n"
      "s_mov_b32 s34, 0x158000\n"
      "s_mov_b32 s35, 0x20000\n"
      "s_mov_b32 s36, s18\n"
      "s_and_b32 s37, s19, 0xffff\n"
      "s_mov_b32 s38, 0x560000\n"
      "s_mov_b32 s39, 0x20000\n"
      "s_mov_b32 s48, s18\n"
      "s_and_b32 s49, s19, 0xffff\n"
      "s_and_b32 s50, s75, 0x560000\n"
      "s_mov_b32 s51, 0x20000\n"
      "s_mov_b32 s52, s92\n"
      "s_and_b32 s53, s93, 0xffff\n"
      "s_and_b32 s54, s67, 0x16000\n"
      "s_mov_b32 s55, 0x20000\n"
      "s_mov_b32 s40, s92\n"
      "s_and_b32 s41, s93, 0xffff\n"
      "s_mov_b32 s42, 0x1e080\n"
      "s_mov_b32 s43, 0x20000\n"
      "s_lshl_b32 s31, s84, 7\n"
      "s_add_u32 s31, s31, 0x16000\n"
      "s_mov_b32 s44, s16\n"
      "s_and_b32 s45, s17, 0xffff\n"
      "s_mov_b32 s46, 0x200000\n"
      "s_mov_b32 s47, 0x20000\n"
      "s_waitcnt lgkmcnt(0)\n"
      "s_mul_i32 s70, s68, 0x5851f42d\n"
      "s_mul_i32 s71, s69, 0x4c957f2d\n"
      "s_mul_hi_u32 s72, s68, 0x4c957f2d\n"
      "s_mul_i32 s73, s68, 0x4c957f2d\n"
      "s_add_u32 s70, s70, s71\n"
      "s_add_u32 s70, s70, s72\n"
      "s_add_u32 s68, s73, 0xf767814f\n"
      "s_addc_u32 s69, s70, 0x14057b7e\n"
      "v_mov_b32 v12, s68\n"
      "v_mov_b32 v13, s69\n"
      "s_barrier\n"
      "ds_read_b64_tr_b16 v[128:129], v6 offset:0\n"
      "ds_read_b64_tr_b16 v[130:131], v6 offset:512\n"
      "ds_read_b64_tr_b16 v[132:133], v6 offset:8192\n"
      "ds_read_b64_tr_b16 v[134:135], v6 offset:8704\n"
      "ds_read_b64_tr_b16 v[136:137], v6 offset:4096\n"
      "ds_read_b64_tr_b16 v[138:139], v6 offset:4608\n"
      "ds_read_b64_tr_b16 v[140:141], v6 offset:12288\n"
      "ds_read_b64_tr_b16 v[142:143], v6 offset:12800\n"
      "ds_read_b64_tr_b16 v[144:145], v6 offset:16384\n"
      "ds_read_b64_tr_b16 v[146:147], v6 offset:16896\n"
      "ds_read_b64_tr_b16 v[148:149], v6 offset:24576\n"
      "ds_read_b64_tr_b16 v[150:151], v6 offset:25088\n"
      "ds_read_b64_tr_b16 v[152:153], v6 offset:20480\n"
      "ds_read_b64_tr_b16 v[154:155], v6 offset:20992\n"
      "s_waitcnt lgkmcnt(7)\n"
      "ds_read_b64_tr_b16 v[156:157], v6 offset:28672\n"
      "ds_read_b64_tr_b16 v[158:159], v6 offset:29184\n"
      "ds_read_b64_tr_b16 v[160:161], v6 offset:32768\n"
      "ds_read_b64_tr_b16 v[162:163], v6 offset:33280\n"
      "ds_read_b64_tr_b16 v[164:165], v6 offset:40960\n"
      "ds_read_b64_tr_b16 v[166:167], v6 offset:41472\n"
      "ds_read_b64_tr_b16 v[168:169], v6 offset:36864\n"
      "s_waitcnt lgkmcnt(7)\n"
      "ds_read_b64_tr_b16 v[170:171], v6 offset:37376\n"
      "ds_read_b64_tr_b16 v[172:173], v6 offset:45056\n"
      "ds_read_b64_tr_b16 v[174:175], v6 offset:45568\n"
      "ds_read_b64_tr_b16 v[176:177], v6 offset:49152\n"
      "ds_read_b64_tr_b16 v[178:179], v6 offset:49664\n"
      "ds_read_b64_tr_b16 v[180:181], v6 offset:57344\n"
      "ds_read_b64_tr_b16 v[182:183], v6 offset:57856\n"
      "s_waitcnt lgkmcnt(7)\n"
      "ds_read_b64_tr_b16 v[184:185], v6 offset:53248\n"
      "ds_read_b64_tr_b16 v[186:187], v6 offset:53760\n"
      "ds_read_b64_tr_b16 v[188:189], v6 offset:61440\n"
      "ds_read_b64_tr_b16 v[190:191], v6 offset:61952\n"
      "s_waitcnt lgkmcnt(0)\n"
      "s_barrier\n"
      "ds_read_b128 v[64:67], v4 offset:0\n"
      "ds_read_b128 v[68:71], v4 offset:2048\n"
      "ds_read_b128 v[72:75], v5 offset:0\n"
      "ds_read_b128 v[76:79], v5 offset:2048\n"
      "ds_read_b128 v[80:83], v4 offset:32768\n"
      "ds_read_b128 v[84:87], v4 offset:34816\n"
      "ds_read_b128 v[88:91], v5 offset:32768\n"
      "ds_read_b128 v[92:95], v5 offset:34816\n"
      "s_waitcnt lgkmcnt(7)\n"
      "v_mfma_f32_16x16x32_bf16 v[96:99], v[128:131], v[64:67], 0\n"
      "ds_read_b128 v[32:35], v4 offset:4096\n"
      "v_mfma_f32_16x16x32_bf16 v[104:107], v[132:135], v[64:67], 0\n"
      "ds_read_b128 v[36:39], v4 offset:6144\n"
      "s_waitcnt lgkmcnt(8)\n"
      "v_mfma_f32_16x16x32_bf16 v[100:103], v[128:131], v[68:71], 0\n"
      "ds_read_b128 v[40:43], v5 offset:4096\n"
      "v_mfma_f32_16x16x32_bf16 v[108:111], v[132:135], v[68:71], 0\n"
      "ds_read_b128 v[44:47], v5 offset:6144\n"
      "s_waitcnt lgkmcnt(9)\n"
      "v_mfma_f32_16x16x32_bf16 v[96:99], v[136:139], v[72:75], v[96:99]\n"
      "ds_read_b128 v[48:51], v4 offset:36864\n"
      "v_mfma_f32_16x16x32_bf16 v[104:107], v[140:143], v[72:75], v[104:107]\n"
      "ds_read_b128 v[52:55], v4 offset:38912\n"
      "s_waitcnt lgkmcnt(10)\n"
      "v_mfma_f32_16x16x32_bf16 v[100:103], v[136:139], v[76:79], v[100:103]\n"
      "ds_read_b128 v[56:59], v5 offset:36864\n"
      "v_mfma_f32_16x16x32_bf16 v[108:111], v[140:143], v[76:79], v[108:111]\n"
      "ds_read_b128 v[60:63], v5 offset:38912\n"
      "s_waitcnt lgkmcnt(11)\n"
      "v_mfma_f32_16x16x32_bf16 v[112:115], v[128:131], v[80:83], 0\n"
      "ds_read_b64_tr_b16 v[192:193], v7 offset:0\n"
      "v_mfma_f32_16x16x32_bf16 v[120:123], v[132:135], v[80:83], 0\n"
      "ds_read_b64_tr_b16 v[194:195], v7 offset:512\n"
      "s_waitcnt lgkmcnt(12)\n"
      "v_mfma_f32_16x16x32_bf16 v[116:119], v[128:131], v[84:87], 0\n"
      "ds_read_b64_tr_b16 v[196:197], v7 offset:8192\n"
      "v_mfma_f32_16x16x32_bf16 v[124:127], v[132:135], v[84:87], 0\n"
      "ds_read_b64_tr_b16 v[198:199], v7 offset:8704\n"
      "s_waitcnt lgkmcnt(13)\n"
      "v_mfma_f32_16x16x32_bf16 v[112:115], v[136:139], v[88:91], v[112:115]\n"
      "ds_read_b64_tr_b16 v[200:201], v7 offset:4096\n"
      "v_mfma_f32_16x16x32_bf16 v[120:123], v[140:143], v[88:91], v[120:123]\n"
      "s_waitcnt lgkmcnt(7)\n"
      "ds_read_b64_tr_b16 v[202:203], v7 offset:4608\n"
      "v_mfma_f32_16x16x32_bf16 v[116:119], v[136:139], v[92:95], v[116:119]\n"
      "ds_read_b64_tr_b16 v[204:205], v7 offset:12288\n"
      "v_mfma_f32_16x16x32_bf16 v[124:127], v[140:143], v[92:95], v[124:127]\n"
      "ds_read_b64_tr_b16 v[206:207], v7 offset:12800\n"
      "v_mfma_f32_16x16x32_bf16 v[96:99], v[144:147], v[32:35], v[96:99]\n"
      "v_mfma_f32_16x16x32_bf16 v[104:107], v[148:151], v[32:35], v[104:107]\n"
      "v_mfma_f32_16x16x32_bf16 v[100:103], v[144:147], v[36:39], v[100:103]\n"
      "v_mfma_f32_16x16x32_bf16 v[108:111], v[148:151], v[36:39], v[108:111]\n"
      "v_mfma_f32_16x16x32_bf16 v[96:99], v[152:155], v[40:43], v[96:99]\n"
      "v_mfma_f32_16x16x32_bf16 v[104:107], v[156:159], v[40:43], v[104:107]\n"
      "v_mfma_f32_16x16x32_bf16 v[100:103], v[152:155], v[44:47], v[100:103]\n"
      "v_mfma_f32_16x16x32_bf16 v[108:111], v[156:159], v[44:47], v[108:111]\n"
      "v_mfma_f32_16x16x32_bf16 v[112:115], v[144:147], v[48:51], v[112:115]\n"
      "v_mfma_f32_16x16x32_bf16 v[120:123], v[148:151], v[48:51], v[120:123]\n"
      "v_mfma_f32_16x16x32_bf16 v[116:119], v[144:147], v[52:55], v[116:119]\n"
      "v_mfma_f32_16x16x32_bf16 v[124:127], v[148:151], v[52:55], v[124:127]\n"
      "s_waitcnt lgkmcnt(9)\n"
      "v_mfma_f32_16x16x32_bf16 v[112:115], v[152:155], v[56:59], v[112:115]\n"
      "v_mfma_f32_16x16x32_bf16 v[120:123], v[156:159], v[56:59], v[120:123]\n"
      "s_waitcnt lgkmcnt(8)\n"
      "v_mfma_f32_16x16x32_bf16 v[116:119], v[152:155], v[60:63], v[116:119]\n"
      "v_mfma_f32_16x16x32_bf16 v[124:127], v[156:159], v[60:63], v[124:127]\n"
      "s_waitcnt lgkmcnt(0)\n"
      "s_barrier\n"
      "ds_read_b128 v[64:67], v4 offset:8192\n"
      "ds_read_b128 v[68:71], v4 offset:10240\n"
      "ds_read_b128 v[72:75], v5 offset:8192\n"
      "ds_read_b128 v[76:79], v5 offset:10240\n"
      "ds_read_b128 v[80:83], v4 offset:40960\n"
      "ds_read_b128 v[84:87], v4 offset:43008\n"
      "ds_read_b128 v[88:91], v5 offset:40960\n"
      "ds_read_b128 v[92:95], v5 offset:43008\n"
      "s_waitcnt lgkmcnt(7)\n"
      "v_mfma_f32_16x16x32_bf16 v[96:99], v[160:163], v[64:67], v[96:99]\n"
      "ds_read_b128 v[32:35], v4 offset:12288\n"
      "v_mfma_f32_16x16x32_bf16 v[104:107], v[164:167], v[64:67], v[104:107]\n"
      "ds_read_b128 v[36:39], v4 offset:14336\n"
      "s_waitcnt lgkmcnt(8)\n"
      "v_mfma_f32_16x16x32_bf16 v[100:103], v[160:163], v[68:71], v[100:103]\n"
      "ds_read_b128 v[40:43], v5 offset:12288\n"
      "v_mfma_f32_16x16x32_bf16 v[108:111], v[164:167], v[68:71], v[108:111]\n"
      "ds_read_b128 v[44:47], v5 offset:14336\n"
      "s_waitcnt lgkmcnt(9)\n"
      "v_mfma_f32_16x16x32_bf16 v[96:99], v[168:171], v[72:75], v[96:99]\n"
      "ds_read_b128 v[48:51], v4 offset:45056\n"
      "v_mfma_f32_16x16x32_bf16 v[104:107], v[172:175], v[72:75], v[104:107]\n"
      "ds_read_b128 v[52:55], v4 offset:47104\n"
      "s_waitcnt lgkmcnt(10)\n"
      "v_mfma_f32_16x16x32_bf16 v[100:103], v[168:171], v[76:79], v[100:103]\n"
      "ds_read_b128 v[56:59], v5 offset:45056\n"
      "v_mfma_f32_16x16x32_bf16 v[108:111], v[172:175], v[76:79], v[108:111]\n"
      "ds_read_b128 v[60:63], v5 offset:47104\n"
      "s_waitcnt lgkmcnt(11)\n"
      "v_mfma_f32_16x16x32_bf16 v[112:115], v[160:163], v[80:83], v[112:115]\n"
      "ds_read_b64_tr_b16 v[208:209], v7 offset:16384\n"
      "v_mfma_f32_16x16x32_bf16 v[120:123], v[164:167], v[80:83], v[120:123]\n"
      "ds_read_b64_tr_b16 v[210:211], v7 offset:16896\n"
      "s_waitcnt lgkmcnt(12)\n"
      "v_mfma_f32_16x16x32_bf16 v[116:119], v[160:163], v[84:87], v[116:119]\n"
      "ds_read_b64_tr_b16 v[212:213], v7 offset:24576\n"
      "v_mfma_f32_16x16x32_bf16 v[124:127], v[164:167], v[84:87], v[124:127]\n"
      "ds_read_b64_tr_b16 v[214:215], v7 offset:25088\n"
      "s_waitcnt lgkmcnt(13)\n"
      "v_mfma_f32_16x16x32_bf16 v[112:115], v[168:171], v[88:91], v[112:115]\n"
      "ds_read_b64_tr_b16 v[216:217], v7 offset:20480\n"
      "v_mfma_f32_16x16x32_bf16 v[120:123], v[172:175], v[88:91], v[120:123]\n"
      "s_waitcnt lgkmcnt(7)\n"
      "ds_read_b64_tr_b16 v[218:219], v7 offset:20992\n"
      "v_mfma_f32_16x16x32_bf16 v[116:119], v[168:171], v[92:95], v[116:119]\n"
      "ds_read_b64_tr_b16 v[220:221], v7 offset:28672\n"
      "v_mfma_f32_16x16x32_bf16 v[124:127], v[172:175], v[92:95], v[124:127]\n"
      "ds_read_b64_tr_b16 v[222:223], v7 offset:29184\n"
      "v_mfma_f32_16x16x32_bf16 v[96:99], v[176:179], v[32:35], v[96:99]\n"
      "v_mfma_f32_16x16x32_bf16 v[104:107], v[180:183], v[32:35], v[104:107]\n"
      "v_mfma_f32_16x16x32_bf16 v[100:103], v[176:179], v[36:39], v[100:103]\n"
      "v_mfma_f32_16x16x32_bf16 v[108:111], v[180:183], v[36:39], v[108:111]\n"
      "v_mfma_f32_16x16x32_bf16 v[96:99], v[184:187], v[40:43], v[96:99]\n"
      "v_mfma_f32_16x16x32_bf16 v[104:107], v[188:191], v[40:43], v[104:107]\n"
      "v_mfma_f32_16x16x32_bf16 v[100:103], v[184:187], v[44:47], v[100:103]\n"
      "v_mfma_f32_16x16x32_bf16 v[108:111], v[188:191], v[44:47], v[108:111]\n"
      "v_mfma_f32_16x16x32_bf16 v[112:115], v[176:179], v[48:51], v[112:115]\n"
      "v_mfma_f32_16x16x32_bf16 v[120:123], v[180:183], v[48:51], v[120:123]\n"
      "v_mfma_f32_16x16x32_bf16 v[116:119], v[176:179], v[52:55], v[116:119]\n"
      "v_mfma_f32_16x16x32_bf16 v[124:127], v[180:183], v[52:55], v[124:127]\n"
      "s_waitcnt lgkmcnt(9)\n"
      "v_mfma_f32_16x16x32_bf16 v[112:115], v[184:187], v[56:59], v[112:115]\n"
      "v_mfma_f32_16x16x32_bf16 v[120:123], v[188:191], v[56:59], v[120:123]\n"
      "s_waitcnt lgkmcnt(8)\n"
      "v_mfma_f32_16x16x32_bf16 v[116:119], v[184:187], v[60:63], v[116:119]\n"
      "v_mfma_f32_16x16x32_bf16 v[124:127], v[188:191], v[60:63], v[124:127]\n"
      "s_waitcnt lgkmcnt(0)\n"
      "s_barrier\n"
      "ds_read_b128 v[64:67], v4 offset:16384\n"
      "ds_read_b128 v[68:71], v4 offset:18432\n"
      "ds_read_b128 v[72:75], v5 offset:16384\n"
      "ds_read_b128 v[76:79], v5 offset:18432\n"
      "ds_read_b128 v[80:83], v4 offset:49152\n"
      "ds_read_b128 v[84:87], v4 offset:51200\n"
      "ds_read_b128 v[88:91], v5 offset:49152\n"
      "ds_read_b128 v[92:95], v5 offset:51200\n"
      "s_waitcnt lgkmcnt(7)\n"
      "v_mfma_f32_16x16x32_bf16 v[96:99], v[192:195], v[64:67], v[96:99]\n"
      "ds_read_b128 v[32:35], v4 offset:20480\n"
      "v_mfma_f32_16x16x32_bf16 v[104:107], v[196:199], v[64:67], v[104:107]\n"
      "ds_read_b128 v[36:39], v4 offset:22528\n"
      "s_waitcnt lgkmcnt(8)\n"
      "v_mfma_f32_16x16x32_bf16 v[100:103], v[192:195], v[68:71], v[100:103]\n"
      "ds_read_b128 v[40:43], v5 offset:20480\n"
      "v_mfma_f32_16x16x32_bf16 v[108:111], v[196:199], v[68:71], v[108:111]\n"
      "ds_read_b128 v[44:47], v5 offset:22528\n"
      "s_waitcnt lgkmcnt(9)\n"
      "v_mfma_f32_16x16x32_bf16 v[96:99], v[200:203], v[72:75], v[96:99]\n"
      "ds_read_b128 v[48:51], v4 offset:53248\n"
      "v_mfma_f32_16x16x32_bf16 v[104:107], v[204:207], v[72:75], v[104:107]\n"
      "ds_read_b128 v[52:55], v4 offset:55296\n"
      "s_waitcnt lgkmcnt(10)\n"
      "v_mfma_f32_16x16x32_bf16 v[100:103], v[200:203], v[76:79], v[100:103]\n"
      "ds_read_b128 v[56:59], v5 offset:53248\n"
      "v_mfma_f32_16x16x32_bf16 v[108:111], v[204:207], v[76:79], v[108:111]\n"
      "ds_read_b128 v[60:63], v5 offset:55296\n"
      "s_waitcnt lgkmcnt(11)\n"
      "v_mfma_f32_16x16x32_bf16 v[112:115], v[192:195], v[80:83], v[112:115]\n"
      "ds_read_b64_tr_b16 v[224:225], v6 offset:0\n"
      "v_mfma_f32_16x16x32_bf16 v[120:123], v[196:199], v[80:83], v[120:123]\n"
      "ds_read_b64_tr_b16 v[226:227], v6 offset:512\n"
      "s_waitcnt lgkmcnt(12)\n"
      "v_mfma_f32_16x16x32_bf16 v[116:119], v[192:195], v[84:87], v[116:119]\n"
      "ds_read_b64_tr_b16 v[228:229], v6 offset:8192\n"
      "v_mfma_f32_16x16x32_bf16 v[124:127], v[196:199], v[84:87], v[124:127]\n"
      "ds_read_b64_tr_b16 v[230:231], v6 offset:8704\n"
      "s_waitcnt lgkmcnt(13)\n"
      "v_mfma_f32_16x16x32_bf16 v[112:115], v[200:203], v[88:91], v[112:115]\n"
      "ds_read_b64_tr_b16 v[232:233], v6 offset:4096\n"
      "v_mfma_f32_16x16x32_bf16 v[120:123], v[204:207], v[88:91], v[120:123]\n"
      "s_waitcnt lgkmcnt(7)\n"
      "ds_read_b64_tr_b16 v[234:235], v6 offset:4608\n"
      "v_mfma_f32_16x16x32_bf16 v[116:119], v[200:203], v[92:95], v[116:119]\n"
      "ds_read_b64_tr_b16 v[236:237], v6 offset:12288\n"
      "v_mfma_f32_16x16x32_bf16 v[124:127], v[204:207], v[92:95], v[124:127]\n"
      "ds_read_b64_tr_b16 v[238:239], v6 offset:12800\n"
      "v_mfma_f32_16x16x32_bf16 v[96:99], v[208:211], v[32:35], v[96:99]\n"
      "v_mfma_f32_16x16x32_bf16 v[104:107], v[212:215], v[32:35], v[104:107]\n"
      "v_mfma_f32_16x16x32_bf16 v[100:103], v[208:211], v[36:39], v[100:103]\n"
      "v_mfma_f32_16x16x32_bf16 v[108:111], v[212:215], v[36:39], v[108:111]\n"
      "v_mfma_f32_16x16x32_bf16 v[96:99], v[216:219], v[40:43], v[96:99]\n"
      "v_mfma_f32_16x16x32_bf16 v[104:107], v[220:223], v[40:43], v[104:107]\n"
      "v_mfma_f32_16x16x32_bf16 v[100:103], v[216:219], v[44:47], v[100:103]\n"
      "v_mfma_f32_16x16x32_bf16 v[108:111], v[220:223], v[44:47], v[108:111]\n"
      "v_mfma_f32_16x16x32_bf16 v[112:115], v[208:211], v[48:51], v[112:115]\n"
      "v_mfma_f32_16x16x32_bf16 v[120:123], v[212:215], v[48:51], v[120:123]\n"
      "v_mfma_f32_16x16x32_bf16 v[116:119], v[208:211], v[52:55], v[116:119]\n"
      "v_mfma_f32_16x16x32_bf16 v[124:127], v[212:215], v[52:55], v[124:127]\n"
      "s_waitcnt lgkmcnt(9)\n"
      "v_mfma_f32_16x16x32_bf16 v[112:115], v[216:219], v[56:59], v[112:115]\n"
      "v_mfma_f32_16x16x32_bf16 v[120:123], v[220:223], v[56:59], v[120:123]\n"
      "s_waitcnt lgkmcnt(8)\n"
      "v_mfma_f32_16x16x32_bf16 v[116:119], v[216:219], v[60:63], v[116:119]\n"
      "v_mfma_f32_16x16x32_bf16 v[124:127], v[220:223], v[60:63], v[124:127]\n"
      "s_waitcnt lgkmcnt(0)\n"
      "s_barrier\n"
      "ds_read_b128 v[64:67], v4 offset:24576\n"
      "ds_read_b128 v[68:71], v4 offset:26624\n"
      "ds_read_b128 v[72:75], v5 offset:24576\n"
      "ds_read_b128 v[76:79], v5 offset:26624\n"
      "ds_read_b128 v[80:83], v4 offset:57344\n"
      "ds_read_b128 v[84:87], v4 offset:59392\n"
      "ds_read_b128 v[88:91], v5 offset:57344\n"
      "ds_read_b128 v[92:95], v5 offset:59392\n"
      "s_waitcnt lgkmcnt(7)\n"
      "v_mfma_f32_16x16x32_bf16 v[96:99], v[224:227], v[64:67], v[96:99]\n"
      "ds_read_b64_tr_b16 v[240:241], v6 offset:16384\n"
      "v_mfma_f32_16x16x32_bf16 v[104:107], v[228:231], v[64:67], v[104:107]\n"
      "ds_read_b64_tr_b16 v[242:243], v6 offset:16896\n"
      "s_waitcnt lgkmcnt(8)\n"
      "v_mfma_f32_16x16x32_bf16 v[100:103], v[224:227], v[68:71], v[100:103]\n"
      "ds_read_b64_tr_b16 v[244:245], v6 offset:24576\n"
      "v_mfma_f32_16x16x32_bf16 v[108:111], v[228:231], v[68:71], v[108:111]\n"
      "ds_read_b64_tr_b16 v[246:247], v6 offset:25088\n"
      "s_waitcnt lgkmcnt(9)\n"
      "v_mfma_f32_16x16x32_bf16 v[96:99], v[232:235], v[72:75], v[96:99]\n"
      "ds_read_b64_tr_b16 v[248:249], v6 offset:20480\n"
      "v_mfma_f32_16x16x32_bf16 v[104:107], v[236:239], v[72:75], v[104:107]\n"
      "ds_read_b64_tr_b16 v[250:251], v6 offset:20992\n"
      "s_waitcnt lgkmcnt(10)\n"
      "v_mfma_f32_16x16x32_bf16 v[100:103], v[232:235], v[76:79], v[100:103]\n"
      "ds_read_b64_tr_b16 v[252:253], v6 offset:28672\n"
      "v_mfma_f32_16x16x32_bf16 v[108:111], v[236:239], v[76:79], v[108:111]\n"
      "ds_read_b64_tr_b16 v[254:255], v6 offset:29184\n"
      "s_waitcnt lgkmcnt(11)\n"
      "v_mfma_f32_16x16x32_bf16 v[112:115], v[224:227], v[80:83], v[112:115]\n"
      "ds_read_b128 v[32:35], v4 offset:28672\n"
      "v_mfma_f32_16x16x32_bf16 v[120:123], v[228:231], v[80:83], v[120:123]\n"
      "ds_read_b128 v[36:39], v4 offset:30720\n"
      "s_waitcnt lgkmcnt(12)\n"
      "v_mfma_f32_16x16x32_bf16 v[116:119], v[224:227], v[84:87], v[116:119]\n"
      "ds_read_b128 v[40:43], v5 offset:28672\n"
      "v_mfma_f32_16x16x32_bf16 v[124:127], v[228:231], v[84:87], v[124:127]\n"
      "ds_read_b128 v[44:47], v5 offset:30720\n"
      "s_waitcnt lgkmcnt(13)\n"
      "v_mfma_f32_16x16x32_bf16 v[112:115], v[232:235], v[88:91], v[112:115]\n"
      "ds_read_b128 v[48:51], v4 offset:61440\n"
      "v_mfma_f32_16x16x32_bf16 v[120:123], v[236:239], v[88:91], v[120:123]\n"
      "s_waitcnt lgkmcnt(7)\n"
      "ds_read_b128 v[52:55], v4 offset:63488\n"
      "v_mfma_f32_16x16x32_bf16 v[116:119], v[232:235], v[92:95], v[116:119]\n"
      "ds_read_b128 v[56:59], v5 offset:61440\n"
      "v_mfma_f32_16x16x32_bf16 v[124:127], v[236:239], v[92:95], v[124:127]\n"
      "ds_read_b128 v[60:63], v5 offset:63488\n"
      "s_waitcnt lgkmcnt(7)\n"
      "v_mfma_f32_16x16x32_bf16 v[96:99], v[240:243], v[32:35], v[96:99]\n"
      "v_mfma_f32_16x16x32_bf16 v[104:107], v[244:247], v[32:35], v[104:107]\n"
      "s_waitcnt lgkmcnt(6)\n"
      "v_mfma_f32_16x16x32_bf16 v[100:103], v[240:243], v[36:39], v[100:103]\n"
      "v_mfma_f32_16x16x32_bf16 v[108:111], v[244:247], v[36:39], v[108:111]\n"
      "s_waitcnt lgkmcnt(5)\n"
      "v_mfma_f32_16x16x32_bf16 v[96:99], v[248:251], v[40:43], v[96:99]\n"
      "v_mfma_f32_16x16x32_bf16 v[104:107], v[252:255], v[40:43], v[104:107]\n"
      "s_waitcnt lgkmcnt(4)\n"
      "v_mfma_f32_16x16x32_bf16 v[100:103], v[248:251], v[44:47], v[100:103]\n"
      "v_mfma_f32_16x16x32_bf16 v[108:111], v[252:255], v[44:47], v[108:111]\n"
      "s_waitcnt lgkmcnt(0)\n"
      "v_add_u32 v4, 98304, v4\n"
      "v_add_u32 v5, 98304, v5\n"
      "s_barrier\n"
      "v_mfma_f32_16x16x32_bf16 v[112:115], v[240:243], v[48:51], v[112:115]\n"
      "ds_read_b128 v[64:67], v4 offset:0\n"
      "v_mfma_f32_16x16x32_bf16 v[120:123], v[244:247], v[48:51], v[120:123]\n"
      "ds_read_b128 v[68:71], v4 offset:2048\n"
      "v_mfma_f32_16x16x32_bf16 v[116:119], v[240:243], v[52:55], v[116:119]\n"
      "ds_read_b128 v[72:75], v5 offset:0\n"
      "v_mfma_f32_16x16x32_bf16 v[124:127], v[244:247], v[52:55], v[124:127]\n"
      "ds_read_b128 v[76:79], v5 offset:2048\n"
      "v_mfma_f32_16x16x32_bf16 v[112:115], v[248:251], v[56:59], v[112:115]\n"
      "ds_read_b128 v[80:83], v4 offset:4096\n"
      "v_mfma_f32_16x16x32_bf16 v[120:123], v[252:255], v[56:59], v[120:123]\n"
      "ds_read_b128 v[84:87], v4 offset:6144\n"
      "v_mfma_f32_16x16x32_bf16 v[116:119], v[248:251], v[60:63], v[116:119]\n"
      "ds_read_b128 v[88:91], v5 offset:4096\n"
      "v_mfma_f32_16x16x32_bf16 v[124:127], v[252:255], v[60:63], v[124:127]\n"
      "v_mul_f32 v104, v96, v104\n"
      "v_mul_f32 v105, v97, v105\n"
      "v_mul_f32 v108, v100, v108\n"
      "v_mul_f32 v109, v101, v109\n"
      "v_mul_f32 v96, s85, v96\n"
      "v_mul_f32 v97, s85, v97\n"
      "v_mul_f32 v100, s85, v100\n"
      "v_mul_f32 v101, s85, v101\n"
      "v_exp_f32 v96, v96\n"
      "v_exp_f32 v97, v97\n"
      "v_exp_f32 v100, v100\n"
      "v_exp_f32 v101, v101\n"
      "v_add_f32 v96, 1.0, v96\n"
      "v_add_f32 v97, 1.0, v97\n"
      "v_add_f32 v100, 1.0, v100\n"
      "v_add_f32 v101, 1.0, v101\n"
      "v_rcp_f32 v96, v96\n"
      "v_rcp_f32 v97, v97\n"
      "v_rcp_f32 v100, v100\n"
      "v_rcp_f32 v101, v101\n"
      "v_mul_f32 v96, v96, v104\n"
      "v_mul_f32 v97, v97, v105\n"
      "v_mul_f32 v100, v100, v108\n"
      "v_mul_f32 v101, v101, v109\n"
      "v_cvt_pk_bf16_f32 v20, v96, v97\n"
      "v_cvt_pk_bf16_f32 v22, v100, v101\n"
      "v_mul_f32 v106, v98, v106\n"
      "v_mul_f32 v107, v99, v107\n"
      "v_mul_f32 v110, v102, v110\n"
      "v_mul_f32 v111, v103, v111\n"
      "v_permlane16_swap_b32 v20, v22\n"
      "v_mul_f32 v98, s85, v98\n"
      "v_mul_f32 v99, s85, v99\n"
      "v_mul_f32 v102, s85, v102\n"
      "v_mul_f32 v103, s85, v103\n"
      "v_exp_f32 v98, v98\n"
      "v_exp_f32 v99, v99\n"
      "v_exp_f32 v102, v102\n"
      "v_exp_f32 v103, v103\n"
      "v_add_f32 v98, 1.0, v98\n"
      "v_add_f32 v99, 1.0, v99\n"
      "v_add_f32 v102, 1.0, v102\n"
      "v_add_f32 v103, 1.0, v103\n"
      "v_rcp_f32 v98, v98\n"
      "v_rcp_f32 v99, v99\n"
      "v_rcp_f32 v102, v102\n"
      "v_rcp_f32 v103, v103\n"
      "v_mul_f32 v98, v98, v106\n"
      "v_mul_f32 v99, v99, v107\n"
      "v_mul_f32 v102, v102, v110\n"
      "v_mul_f32 v103, v103, v111\n"
      "v_cvt_pk_bf16_f32 v21, v98, v99\n"
      "v_cvt_pk_bf16_f32 v23, v102, v103\n"
      "s_nop 1\n"
      "v_permlane16_swap_b32 v21, v23\n"
      "s_waitcnt lgkmcnt(4)\n"
      "v_mfma_f32_16x16x32_bf16 v[96:99], v[128:131], v[64:67], 0\n"
      "ds_read_b128 v[92:95], v5 offset:6144\n"
      "v_mfma_f32_16x16x32_bf16 v[104:107], v[132:135], v[64:67], 0\n"
      "v_mfma_f32_16x16x32_bf16 v[100:103], v[128:131], v[68:71], 0\n"
      "ds_read_b128 v[64:67], v4 offset:8192\n"
      "s_waitcnt lgkmcnt(4)\n"
      "v_mfma_f32_16x16x32_bf16 v[108:111], v[132:135], v[68:71], 0\n"
      "v_mfma_f32_16x16x32_bf16 v[96:99], v[136:139], v[72:75], v[96:99]\n"
      "ds_read_b128 v[68:71], v4 offset:10240\n"
      "v_mul_f32 v120, v112, v120\n"
      "v_mfma_f32_16x16x32_bf16 v[104:107], v[140:143], v[72:75], v[104:107]\n"
      "v_mul_f32 v112, s85, v112\n"
      "v_mfma_f32_16x16x32_bf16 v[100:103], v[136:139], v[76:79], v[100:103]\n"
      "ds_read_b128 v[72:75], v5 offset:8192\n"
      "s_waitcnt lgkmcnt(4)\n"
      "v_mfma_f32_16x16x32_bf16 v[108:111], v[140:143], v[76:79], v[108:111]\n"
      "v_exp_f32 v112, v112\n"
      "v_mfma_f32_16x16x32_bf16 v[96:99], v[144:147], v[80:83], v[96:99]\n"
      "ds_read_b128 v[76:79], v5 offset:10240\n"
      "v_add_f32 v112, 1.0, v112\n"
      "v_mfma_f32_16x16x32_bf16 v[104:107], v[148:151], v[80:83], v[104:107]\n"
      "v_rcp_f32 v112, v112\n"
      "v_mfma_f32_16x16x32_bf16 v[100:103], v[144:147], v[84:87], v[100:103]\n"
      "ds_read_b128 v[80:83], v4 offset:12288\n"
      "v_mul_f32 v121, v113, v121\n"
      "v_mfma_f32_16x16x32_bf16 v[108:111], v[148:151], v[84:87], v[108:111]\n"
      "v_mul_f32 v112, v112, v120\n"
      "v_mul_f32 v113, s85, v113\n"
      "v_mfma_f32_16x16x32_bf16 v[96:99], v[152:155], v[88:91], v[96:99]\n"
      "ds_read_b128 v[84:87], v4 offset:14336\n"
      "s_waitcnt lgkmcnt(4)\n"
      "v_mfma_f32_16x16x32_bf16 v[104:107], v[156:159], v[88:91], v[104:107]\n"
      "v_exp_f32 v113, v113\n"
      "v_mfma_f32_16x16x32_bf16 v[100:103], v[152:155], v[92:95], v[100:103]\n"
      "ds_read_b128 v[88:91], v5 offset:12288\n"
      "v_add_f32 v113, 1.0, v113\n"
      "v_mfma_f32_16x16x32_bf16 v[108:111], v[156:159], v[92:95], v[108:111]\n"
      "v_rcp_f32 v113, v113\n"
      "v_mfma_f32_16x16x32_bf16 v[96:99], v[160:163], v[64:67], v[96:99]\n"
      "ds_read_b128 v[92:95], v5 offset:14336\n"
      "v_mul_f32 v124, v116, v124\n"
      "v_mfma_f32_16x16x32_bf16 v[104:107], v[164:167], v[64:67], v[104:107]\n"
      "v_mul_f32 v113, v113, v121\n"
      "v_mul_f32 v116, s85, v116\n"
      "v_mfma_f32_16x16x32_bf16 v[100:103], v[160:163], v[68:71], v[100:103]\n"
      "v_cvt_pk_bf16_f32 v16, v112, v113\n"
      "s_waitcnt lgkmcnt(3)\n"
      "v_mfma_f32_16x16x32_bf16 v[108:111], v[164:167], v[68:71], v[108:111]\n"
      "v_exp_f32 v116, v116\n"
      "v_mfma_f32_16x16x32_bf16 v[96:99], v[168:171], v[72:75], v[96:99]\n"
      "s_waitcnt lgkmcnt(0)\n"
      "s_barrier\n"
      "ds_read_b128 v[64:67], v4 offset:16384\n"
      "v_add_f32 v116, 1.0, v116\n"
      "v_mfma_f32_16x16x32_bf16 v[104:107], v[172:175], v[72:75], v[104:107]\n"
      "ds_read_b128 v[68:71], v4 offset:18432\n"
      "v_rcp_f32 v116, v116\n"
      "v_mfma_f32_16x16x32_bf16 v[100:103], v[168:171], v[76:79], v[100:103]\n"
      "ds_read_b128 v[72:75], v5 offset:16384\n"
      "v_mul_f32 v125, v117, v125\n"
      "v_mfma_f32_16x16x32_bf16 v[108:111], v[172:175], v[76:79], v[108:111]\n"
      "v_mul_f32 v116, v116, v124\n"
      "v_mul_f32 v117, s85, v117\n"
      "v_mfma_f32_16x16x32_bf16 v[96:99], v[176:179], v[80:83], v[96:99]\n"
      "ds_read_b128 v[76:79], v5 offset:18432\n"
      "v_mfma_f32_16x16x32_bf16 v[104:107], v[180:183], v[80:83], v[104:107]\n"
      "v_exp_f32 v117, v117\n"
      "v_mfma_f32_16x16x32_bf16 v[100:103], v[176:179], v[84:87], v[100:103]\n"
      "ds_read_b128 v[80:83], v4 offset:20480\n"
      "v_add_f32 v117, 1.0, v117\n"
      "v_mfma_f32_16x16x32_bf16 v[108:111], v[180:183], v[84:87], v[108:111]\n"
      "v_rcp_f32 v117, v117\n"
      "v_mfma_f32_16x16x32_bf16 v[96:99], v[184:187], v[88:91], v[96:99]\n"
      "ds_read_b128 v[84:87], v4 offset:22528\n"
      "v_mul_f32 v122, v114, v122\n"
      "v_mfma_f32_16x16x32_bf16 v[104:107], v[188:191], v[88:91], v[104:107]\n"
      "v_mul_f32 v117, v117, v125\n"
      "v_mul_f32 v114, s85, v114\n"
      "v_mfma_f32_16x16x32_bf16 v[100:103], v[184:187], v[92:95], v[100:103]\n"
      "ds_read_b128 v[88:91], v5 offset:20480\n"
      "v_cvt_pk_bf16_f32 v18, v116, v117\n"
      "v_mfma_f32_16x16x32_bf16 v[108:111], v[188:191], v[92:95], v[108:111]\n"
      "v_exp_f32 v114, v114\n"
      "s_waitcnt lgkmcnt(4)\n"
      "v_mfma_f32_16x16x32_bf16 v[96:99], v[192:195], v[64:67], v[96:99]\n"
      "ds_read_b128 v[92:95], v5 offset:22528\n"
      "v_add_f32 v114, 1.0, v114\n"
      "v_mfma_f32_16x16x32_bf16 v[104:107], v[196:199], v[64:67], v[104:107]\n"
      "v_rcp_f32 v114, v114\n"
      "v_mfma_f32_16x16x32_bf16 v[100:103], v[192:195], v[68:71], v[100:103]\n"
      "ds_read_b128 v[64:67], v4 offset:24576\n"
      "v_mul_f32 v123, v115, v123\n"
      "v_mfma_f32_16x16x32_bf16 v[108:111], v[196:199], v[68:71], v[108:111]\n"
      "v_mul_f32 v114, v114, v122\n"
      "v_mul_f32 v115, s85, v115\n"
      "v_mfma_f32_16x16x32_bf16 v[96:99], v[200:203], v[72:75], v[96:99]\n"
      "ds_read_b128 v[68:71], v4 offset:26624\n"
      "s_waitcnt lgkmcnt(4)\n"
      "v_mfma_f32_16x16x32_bf16 v[104:107], v[204:207], v[72:75], v[104:107]\n"
      "v_exp_f32 v115, v115\n"
      "v_mfma_f32_16x16x32_bf16 v[100:103], v[200:203], v[76:79], v[100:103]\n"
      "ds_read_b128 v[72:75], v5 offset:24576\n"
      "v_add_f32 v115, 1.0, v115\n"
      "v_mfma_f32_16x16x32_bf16 v[108:111], v[204:207], v[76:79], v[108:111]\n"
      "v_rcp_f32 v115, v115\n"
      "v_mfma_f32_16x16x32_bf16 v[96:99], v[208:211], v[80:83], v[96:99]\n"
      "ds_read_b128 v[76:79], v5 offset:26624\n"
      "v_mul_f32 v126, v118, v126\n"
      "v_mfma_f32_16x16x32_bf16 v[104:107], v[212:215], v[80:83], v[104:107]\n"
      "v_mul_f32 v115, v115, v123\n"
      "v_mul_f32 v118, s85, v118\n"
      "v_mfma_f32_16x16x32_bf16 v[100:103], v[208:211], v[84:87], v[100:103]\n"
      "ds_read_b128 v[80:83], v4 offset:28672\n"
      "v_cvt_pk_bf16_f32 v17, v114, v115\n"
      "v_mfma_f32_16x16x32_bf16 v[108:111], v[212:215], v[84:87], v[108:111]\n"
      "v_exp_f32 v118, v118\n"
      "s_waitcnt lgkmcnt(4)\n"
      "v_mfma_f32_16x16x32_bf16 v[96:99], v[216:219], v[88:91], v[96:99]\n"
      "ds_read_b128 v[84:87], v4 offset:30720\n"
      "v_add_f32 v118, 1.0, v118\n"
      "v_mfma_f32_16x16x32_bf16 v[104:107], v[220:223], v[88:91], v[104:107]\n"
      "v_rcp_f32 v118, v118\n"
      "v_mfma_f32_16x16x32_bf16 v[100:103], v[216:219], v[92:95], v[100:103]\n"
      "ds_read_b128 v[88:91], v5 offset:28672\n"
      "v_mul_f32 v127, v119, v127\n"
      "v_mfma_f32_16x16x32_bf16 v[108:111], v[220:223], v[92:95], v[108:111]\n"
      "v_mul_f32 v118, v118, v126\n"
      "v_mul_f32 v119, s85, v119\n"
      "v_mfma_f32_16x16x32_bf16 v[96:99], v[224:227], v[64:67], v[96:99]\n"
      "ds_read_b128 v[92:95], v5 offset:30720\n"
      "s_waitcnt lgkmcnt(4)\n"
      "v_mfma_f32_16x16x32_bf16 v[104:107], v[228:231], v[64:67], v[104:107]\n"
      "v_exp_f32 v119, v119\n"
      "v_mfma_f32_16x16x32_bf16 v[100:103], v[224:227], v[68:71], v[100:103]\n"
      "v_add_f32 v119, 1.0, v119\n"
      "v_mfma_f32_16x16x32_bf16 v[108:111], v[228:231], v[68:71], v[108:111]\n"
      "v_rcp_f32 v119, v119\n"
      "v_mfma_f32_16x16x32_bf16 v[96:99], v[232:235], v[72:75], v[96:99]\n"
      "s_waitcnt lgkmcnt(0)\n"
      "s_barrier\n"
      "ds_read_b128 v[64:67], v4 offset:32768\n"
      "v_mul_f32 v119, v119, v127\n"
      "v_mfma_f32_16x16x32_bf16 v[104:107], v[236:239], v[72:75], v[104:107]\n"
      "ds_read_b128 v[68:71], v4 offset:34816\n"
      "v_cvt_pk_bf16_f32 v19, v118, v119\n"
      "v_mfma_f32_16x16x32_bf16 v[100:103], v[232:235], v[76:79], v[100:103]\n"
      "ds_read_b128 v[72:75], v5 offset:32768\n"
      "v_permlane16_swap_b32 v16, v18\n"
      "v_mfma_f32_16x16x32_bf16 v[108:111], v[236:239], v[76:79], v[108:111]\n"
      "v_permlane16_swap_b32 v17, v19\n"
      "v_mfma_f32_16x16x32_bf16 v[96:99], v[240:243], v[80:83], v[96:99]\n"
      "ds_read_b128 v[76:79], v5 offset:34816\n"
      "v_mfma_f32_16x16x32_bf16 v[104:107], v[244:247], v[80:83], v[104:107]\n"
      "s_cmp_lg_u32 s74, 0\n"
      "s_cbranch_scc1 .Lglu_C_NOSTART\n"
      "buffer_store_dwordx2 v[12:13], v11, s[40:43], s31 offen sc1\n"
      ".Lglu_C_NOSTART:\n"
      "buffer_store_dwordx4 v[20:23], v8, s[48:51], s24 offen sc1\n"
      "buffer_store_dwordx4 v[16:19], v9, s[48:51], s24 offen sc1\n"
      "v_mfma_f32_16x16x32_bf16 v[100:103], v[240:243], v[84:87], v[100:103]\n"
      "ds_read_b128 v[80:83], v4 offset:36864\n"
      "v_mfma_f32_16x16x32_bf16 v[108:111], v[244:247], v[84:87], v[108:111]\n"
      "v_mfma_f32_16x16x32_bf16 v[96:99], v[248:251], v[88:91], v[96:99]\n"
      "ds_read_b128 v[84:87], v4 offset:38912\n"
      "s_waitcnt lgkmcnt(4)\n"
      "v_mfma_f32_16x16x32_bf16 v[104:107], v[252:255], v[88:91], v[104:107]\n"
      "v_mfma_f32_16x16x32_bf16 v[100:103], v[248:251], v[92:95], v[100:103]\n"
      "ds_read_b128 v[88:91], v5 offset:36864\n"
      "v_mfma_f32_16x16x32_bf16 v[108:111], v[252:255], v[92:95], v[108:111]\n"
      "v_mfma_f32_16x16x32_bf16 v[112:115], v[128:131], v[64:67], 0\n"
      "ds_read_b128 v[92:95], v5 offset:38912\n"
      "s_waitcnt lgkmcnt(4)\n"
      "v_mfma_f32_16x16x32_bf16 v[120:123], v[132:135], v[64:67], 0\n"
      "v_mfma_f32_16x16x32_bf16 v[116:119], v[128:131], v[68:71], 0\n"
      "ds_read_b128 v[64:67], v4 offset:40960\n"
      "v_mfma_f32_16x16x32_bf16 v[124:127], v[132:135], v[68:71], 0\n"
      "v_mfma_f32_16x16x32_bf16 v[112:115], v[136:139], v[72:75], v[112:115]\n"
      "ds_read_b128 v[68:71], v4 offset:43008\n"
      "v_mul_f32 v104, v96, v104\n"
      "v_mfma_f32_16x16x32_bf16 v[120:123], v[140:143], v[72:75], v[120:123]\n"
      "v_mul_f32 v96, s85, v96\n"
      "v_mfma_f32_16x16x32_bf16 v[116:119], v[136:139], v[76:79], v[116:119]\n"
      "ds_read_b128 v[72:75], v5 offset:40960\n"
      "s_waitcnt lgkmcnt(4)\n"
      "v_mfma_f32_16x16x32_bf16 v[124:127], v[140:143], v[76:79], v[124:127]\n"
      "v_exp_f32 v96, v96\n"
      "v_mfma_f32_16x16x32_bf16 v[112:115], v[144:147], v[80:83], v[112:115]\n"
      "ds_read_b128 v[76:79], v5 offset:43008\n"
      "v_add_f32 v96, 1.0, v96\n"
      "v_mfma_f32_16x16x32_bf16 v[120:123], v[148:151], v[80:83], v[120:123]\n"
      "v_rcp_f32 v96, v96\n"
      "v_mfma_f32_16x16x32_bf16 v[116:119], v[144:147], v[84:87], v[116:119]\n"
      "ds_read_b128 v[80:83], v4 offset:45056\n"
      "v_mul_f32 v105, v97, v105\n"
      "v_mfma_f32_16x16x32_bf16 v[124:127], v[148:151], v[84:87], v[124:127]\n"
      "v_mul_f32 v96, v96, v104\n"
      "v_mul_f32 v97, s85, v97\n"
      "v_mfma_f32_16x16x32_bf16 v[112:115], v[152:155], v[88:91], v[112:115]\n"
      "ds_read_b128 v[84:87], v4 offset:47104\n"
      "s_waitcnt lgkmcnt(4)\n"
      "v_mfma_f32_16x16x32_bf16 v[120:123], v[156:159], v[88:91], v[120:123]\n"
      "v_exp_f32 v97, v97\n"
      "v_mfma_f32_16x16x32_bf16 v[116:119], v[152:155], v[92:95], v[116:119]\n"
      "ds_read_b128 v[88:91], v5 offset:45056\n"
      "v_add_f32 v97, 1.0, v97\n"
      "v_mfma_f32_16x16x32_bf16 v[124:127], v[156:159], v[92:95], v[124:127]\n"
      "v_rcp_f32 v97, v97\n"
      "v_mfma_f32_16x16x32_bf16 v[112:115], v[160:163], v[64:67], v[112:115]\n"
      "ds_read_b128 v[92:95], v5 offset:47104\n"
      "v_mul_f32 v108, v100, v108\n"
      "v_mfma_f32_16x16x32_bf16 v[120:123], v[164:167], v[64:67], v[120:123]\n"
      "v_mul_f32 v97, v97, v105\n"
      "v_mul_f32 v100, s85, v100\n"
      "v_mfma_f32_16x16x32_bf16 v[116:119], v[160:163], v[68:71], v[116:119]\n"
      "v_cvt_pk_bf16_f32 v16, v96, v97\n"
      "s_waitcnt lgkmcnt(3)\n"
      "v_mfma_f32_16x16x32_bf16 v[124:127], v[164:167], v[68:71], v[124:127]\n"
      "v_exp_f32 v100, v100\n"
      "v_mfma_f32_16x16x32_bf16 v[112:115], v[168:171], v[72:75], v[112:115]\n"
      "s_waitcnt lgkmcnt(0)\n"
      "s_barrier\n"
      "ds_read_b128 v[64:67], v4 offset:49152\n"
      "v_add_f32 v100, 1.0, v100\n"
      "v_mfma_f32_16x16x32_bf16 v[120:123], v[172:175], v[72:75], v[120:123]\n"
      "ds_read_b128 v[68:71], v4 offset:51200\n"
      "v_rcp_f32 v100, v100\n"
      "v_mfma_f32_16x16x32_bf16 v[116:119], v[168:171], v[76:79], v[116:119]\n"
      "ds_read_b128 v[72:75], v5 offset:49152\n"
      "v_mul_f32 v109, v101, v109\n"
      "v_mfma_f32_16x16x32_bf16 v[124:127], v[172:175], v[76:79], v[124:127]\n"
      "v_mul_f32 v100, v100, v108\n"
      "v_mul_f32 v101, s85, v101\n"
      "v_mfma_f32_16x16x32_bf16 v[112:115], v[176:179], v[80:83], v[112:115]\n"
      "ds_read_b128 v[76:79], v5 offset:51200\n"
      "v_mfma_f32_16x16x32_bf16 v[120:123], v[180:183], v[80:83], v[120:123]\n"
      "v_exp_f32 v101, v101\n"
      "v_mfma_f32_16x16x32_bf16 v[116:119], v[176:179], v[84:87], v[116:119]\n"
      "ds_read_b128 v[80:83], v4 offset:53248\n"
      "v_add_f32 v101, 1.0, v101\n"
      "v_mfma_f32_16x16x32_bf16 v[124:127], v[180:183], v[84:87], v[124:127]\n"
      "v_rcp_f32 v101, v101\n"
      "v_mfma_f32_16x16x32_bf16 v[112:115], v[184:187], v[88:91], v[112:115]\n"
      "ds_read_b128 v[84:87], v4 offset:55296\n"
      "v_mul_f32 v106, v98, v106\n"
      "v_mfma_f32_16x16x32_bf16 v[120:123], v[188:191], v[88:91], v[120:123]\n"
      "v_mul_f32 v101, v101, v109\n"
      "v_mul_f32 v98, s85, v98\n"
      "v_mfma_f32_16x16x32_bf16 v[116:119], v[184:187], v[92:95], v[116:119]\n"
      "ds_read_b128 v[88:91], v5 offset:53248\n"
      "v_cvt_pk_bf16_f32 v18, v100, v101\n"
      "v_mfma_f32_16x16x32_bf16 v[124:127], v[188:191], v[92:95], v[124:127]\n"
      "v_exp_f32 v98, v98\n"
      "s_waitcnt lgkmcnt(4)\n"
      "v_mfma_f32_16x16x32_bf16 v[112:115], v[192:195], v[64:67], v[112:115]\n"
      "ds_read_b128 v[92:95], v5 offset:55296\n"
      "v_add_f32 v98, 1.0, v98\n"
      "v_mfma_f32_16x16x32_bf16 v[120:123], v[196:199], v[64:67], v[120:123]\n"
      "v_rcp_f32 v98, v98\n"
      "v_mfma_f32_16x16x32_bf16 v[116:119], v[192:195], v[68:71], v[116:119]\n"
      "ds_read_b128 v[64:67], v4 offset:57344\n"
      "v_mul_f32 v107, v99, v107\n"
      "v_mfma_f32_16x16x32_bf16 v[124:127], v[196:199], v[68:71], v[124:127]\n"
      "v_mul_f32 v98, v98, v106\n"
      "v_mul_f32 v99, s85, v99\n"
      "v_mfma_f32_16x16x32_bf16 v[112:115], v[200:203], v[72:75], v[112:115]\n"
      "ds_read_b128 v[68:71], v4 offset:59392\n"
      "s_waitcnt lgkmcnt(4)\n"
      "v_mfma_f32_16x16x32_bf16 v[120:123], v[204:207], v[72:75], v[120:123]\n"
      "v_exp_f32 v99, v99\n"
      "v_mfma_f32_16x16x32_bf16 v[116:119], v[200:203], v[76:79], v[116:119]\n"
      "ds_read_b128 v[72:75], v5 offset:57344\n"
      "v_add_f32 v99, 1.0, v99\n"
      "v_mfma_f32_16x16x32_bf16 v[124:127], v[204:207], v[76:79], v[124:127]\n"
      "v_rcp_f32 v99, v99\n"
      "v_mfma_f32_16x16x32_bf16 v[112:115], v[208:211], v[80:83], v[112:115]\n"
      "ds_read_b128 v[76:79], v5 offset:59392\n"
      "v_mul_f32 v110, v102, v110\n"
      "v_mfma_f32_16x16x32_bf16 v[120:123], v[212:215], v[80:83], v[120:123]\n"
      "v_mul_f32 v99, v99, v107\n"
      "v_mul_f32 v102, s85, v102\n"
      "v_mfma_f32_16x16x32_bf16 v[116:119], v[208:211], v[84:87], v[116:119]\n"
      "ds_read_b128 v[80:83], v4 offset:61440\n"
      "v_cvt_pk_bf16_f32 v17, v98, v99\n"
      "v_mfma_f32_16x16x32_bf16 v[124:127], v[212:215], v[84:87], v[124:127]\n"
      "v_exp_f32 v102, v102\n"
      "s_waitcnt lgkmcnt(4)\n"
      "v_mfma_f32_16x16x32_bf16 v[112:115], v[216:219], v[88:91], v[112:115]\n"
      "ds_read_b128 v[84:87], v4 offset:63488\n"
      "v_add_f32 v102, 1.0, v102\n"
      "v_mfma_f32_16x16x32_bf16 v[120:123], v[220:223], v[88:91], v[120:123]\n"
      "v_rcp_f32 v102, v102\n"
      "v_mfma_f32_16x16x32_bf16 v[116:119], v[216:219], v[92:95], v[116:119]\n"
      "ds_read_b128 v[88:91], v5 offset:61440\n"
      "v_mul_f32 v111, v103, v111\n"
      "v_mfma_f32_16x16x32_bf16 v[124:127], v[220:223], v[92:95], v[124:127]\n"
      "v_mul_f32 v102, v102, v110\n"
      "v_mul_f32 v103, s85, v103\n"
      "v_mfma_f32_16x16x32_bf16 v[112:115], v[224:227], v[64:67], v[112:115]\n"
      "ds_read_b128 v[92:95], v5 offset:63488\n"
      "s_waitcnt lgkmcnt(4)\n"
      "v_mfma_f32_16x16x32_bf16 v[120:123], v[228:231], v[64:67], v[120:123]\n"
      "v_exp_f32 v103, v103\n"
      "v_mfma_f32_16x16x32_bf16 v[116:119], v[224:227], v[68:71], v[116:119]\n"
      "v_add_f32 v103, 1.0, v103\n"
      "v_mfma_f32_16x16x32_bf16 v[124:127], v[228:231], v[68:71], v[124:127]\n"
      "v_rcp_f32 v103, v103\n"
      "v_mfma_f32_16x16x32_bf16 v[112:115], v[232:235], v[72:75], v[112:115]\n"
      "s_waitcnt lgkmcnt(0)\n"
      "s_barrier\n"
      "ds_read_b128 v[64:67], v4 offset:0\n"
      "v_mul_f32 v103, v103, v111\n"
      "v_mfma_f32_16x16x32_bf16 v[120:123], v[236:239], v[72:75], v[120:123]\n"
      "ds_read_b128 v[68:71], v4 offset:2048\n"
      "v_cvt_pk_bf16_f32 v19, v102, v103\n"
      "v_mfma_f32_16x16x32_bf16 v[116:119], v[232:235], v[76:79], v[116:119]\n"
      "ds_read_b128 v[72:75], v5 offset:0\n"
      "v_permlane16_swap_b32 v16, v18\n"
      "v_mfma_f32_16x16x32_bf16 v[124:127], v[236:239], v[76:79], v[124:127]\n"
      "v_permlane16_swap_b32 v17, v19\n"
      "v_mfma_f32_16x16x32_bf16 v[112:115], v[240:243], v[80:83], v[112:115]\n"
      "ds_read_b128 v[76:79], v5 offset:2048\n"
      "v_mfma_f32_16x16x32_bf16 v[120:123], v[244:247], v[80:83], v[120:123]\n"
      "s_waitcnt vmcnt(0)\n"
      "buffer_store_dwordx2 v[12:13], v11, s[52:55], s28 offen sc1\n"
      "buffer_store_dwordx4 v[16:19], v8, s[48:51], s25 offen sc1\n"
      "v_mfma_f32_16x16x32_bf16 v[116:119], v[240:243], v[84:87], v[116:119]\n"
      "ds_read_b128 v[80:83], v4 offset:4096\n"
      "v_mfma_f32_16x16x32_bf16 v[124:127], v[244:247], v[84:87], v[124:127]\n"
      "v_mfma_f32_16x16x32_bf16 v[112:115], v[248:251], v[88:91], v[112:115]\n"
      "ds_read_b128 v[84:87], v4 offset:6144\n"
      "s_waitcnt lgkmcnt(4)\n"
      "v_mfma_f32_16x16x32_bf16 v[120:123], v[252:255], v[88:91], v[120:123]\n"
      "v_mfma_f32_16x16x32_bf16 v[116:119], v[248:251], v[92:95], v[116:119]\n"
      "ds_read_b128 v[88:91], v5 offset:4096\n"
      "v_mfma_f32_16x16x32_bf16 v[124:127], v[252:255], v[92:95], v[124:127]\n"
      "v_mfma_f32_16x16x32_bf16 v[96:99], v[128:131], v[64:67], 0\n"
      "ds_read_b128 v[92:95], v5 offset:6144\n"
      "s_waitcnt lgkmcnt(4)\n"
      "v_mfma_f32_16x16x32_bf16 v[104:107], v[132:135], v[64:67], 0\n"
      "v_mfma_f32_16x16x32_bf16 v[100:103], v[128:131], v[68:71], 0\n"
      "ds_read_b128 v[64:67], v4 offset:8192\n"
      "v_mfma_f32_16x16x32_bf16 v[108:111], v[132:135], v[68:71], 0\n"
      "v_mfma_f32_16x16x32_bf16 v[96:99], v[136:139], v[72:75], v[96:99]\n"
      "ds_read_b128 v[68:71], v4 offset:10240\n"
      "v_mul_f32 v120, v112, v120\n"
      "v_mfma_f32_16x16x32_bf16 v[104:107], v[140:143], v[72:75], v[104:107]\n"
      "v_mul_f32 v112, s85, v112\n"
      "v_mfma_f32_16x16x32_bf16 v[100:103], v[136:139], v[76:79], v[100:103]\n"
      "ds_read_b128 v[72:75], v5 offset:8192\n"
      "s_waitcnt lgkmcnt(4)\n"
      "v_mfma_f32_16x16x32_bf16 v[108:111], v[140:143], v[76:79], v[108:111]\n"
      "v_exp_f32 v112, v112\n"
      "v_mfma_f32_16x16x32_bf16 v[96:99], v[144:147], v[80:83], v[96:99]\n"
      "ds_read_b128 v[76:79], v5 offset:10240\n"
      "v_add_f32 v112, 1.0, v112\n"
      "v_mfma_f32_16x16x32_bf16 v[104:107], v[148:151], v[80:83], v[104:107]\n"
      "v_rcp_f32 v112, v112\n"
      "v_mfma_f32_16x16x32_bf16 v[100:103], v[144:147], v[84:87], v[100:103]\n"
      "ds_read_b128 v[80:83], v4 offset:12288\n"
      "v_mul_f32 v121, v113, v121\n"
      "v_mfma_f32_16x16x32_bf16 v[108:111], v[148:151], v[84:87], v[108:111]\n"
      "v_mul_f32 v112, v112, v120\n"
      "v_mul_f32 v113, s85, v113\n"
      "v_mfma_f32_16x16x32_bf16 v[96:99], v[152:155], v[88:91], v[96:99]\n"
      "ds_read_b128 v[84:87], v4 offset:14336\n"
      "s_waitcnt lgkmcnt(4)\n"
      "v_mfma_f32_16x16x32_bf16 v[104:107], v[156:159], v[88:91], v[104:107]\n"
      "v_exp_f32 v113, v113\n"
      "v_mfma_f32_16x16x32_bf16 v[100:103], v[152:155], v[92:95], v[100:103]\n"
      "ds_read_b128 v[88:91], v5 offset:12288\n"
      "v_add_f32 v113, 1.0, v113\n"
      "v_mfma_f32_16x16x32_bf16 v[108:111], v[156:159], v[92:95], v[108:111]\n"
      "v_rcp_f32 v113, v113\n"
      "v_mfma_f32_16x16x32_bf16 v[96:99], v[160:163], v[64:67], v[96:99]\n"
      "ds_read_b128 v[92:95], v5 offset:14336\n"
      "v_mul_f32 v124, v116, v124\n"
      "v_mfma_f32_16x16x32_bf16 v[104:107], v[164:167], v[64:67], v[104:107]\n"
      "v_mul_f32 v113, v113, v121\n"
      "v_mul_f32 v116, s85, v116\n"
      "v_mfma_f32_16x16x32_bf16 v[100:103], v[160:163], v[68:71], v[100:103]\n"
      "v_cvt_pk_bf16_f32 v16, v112, v113\n"
      "s_waitcnt lgkmcnt(3)\n"
      "v_mfma_f32_16x16x32_bf16 v[108:111], v[164:167], v[68:71], v[108:111]\n"
      "v_exp_f32 v116, v116\n"
      "v_mfma_f32_16x16x32_bf16 v[96:99], v[168:171], v[72:75], v[96:99]\n"
      "s_waitcnt lgkmcnt(0)\n"
      "s_barrier\n"
      "ds_read_b128 v[64:67], v4 offset:16384\n"
      "v_add_f32 v116, 1.0, v116\n"
      "v_mfma_f32_16x16x32_bf16 v[104:107], v[172:175], v[72:75], v[104:107]\n"
      "ds_read_b128 v[68:71], v4 offset:18432\n"
      "v_rcp_f32 v116, v116\n"
      "v_mfma_f32_16x16x32_bf16 v[100:103], v[168:171], v[76:79], v[100:103]\n"
      "ds_read_b128 v[72:75], v5 offset:16384\n"
      "v_mul_f32 v125, v117, v125\n"
      "v_mfma_f32_16x16x32_bf16 v[108:111], v[172:175], v[76:79], v[108:111]\n"
      "v_mul_f32 v116, v116, v124\n"
      "v_mul_f32 v117, s85, v117\n"
      "v_mfma_f32_16x16x32_bf16 v[96:99], v[176:179], v[80:83], v[96:99]\n"
      "ds_read_b128 v[76:79], v5 offset:18432\n"
      "v_mfma_f32_16x16x32_bf16 v[104:107], v[180:183], v[80:83], v[104:107]\n"
      "v_exp_f32 v117, v117\n"
      "v_mfma_f32_16x16x32_bf16 v[100:103], v[176:179], v[84:87], v[100:103]\n"
      "ds_read_b128 v[80:83], v4 offset:20480\n"
      "v_add_f32 v117, 1.0, v117\n"
      "v_mfma_f32_16x16x32_bf16 v[108:111], v[180:183], v[84:87], v[108:111]\n"
      "v_rcp_f32 v117, v117\n"
      "v_mfma_f32_16x16x32_bf16 v[96:99], v[184:187], v[88:91], v[96:99]\n"
      "ds_read_b128 v[84:87], v4 offset:22528\n"
      "v_mul_f32 v122, v114, v122\n"
      "v_mfma_f32_16x16x32_bf16 v[104:107], v[188:191], v[88:91], v[104:107]\n"
      "v_mul_f32 v117, v117, v125\n"
      "v_mul_f32 v114, s85, v114\n"
      "v_mfma_f32_16x16x32_bf16 v[100:103], v[184:187], v[92:95], v[100:103]\n"
      "ds_read_b128 v[88:91], v5 offset:20480\n"
      "v_cvt_pk_bf16_f32 v18, v116, v117\n"
      "v_mfma_f32_16x16x32_bf16 v[108:111], v[188:191], v[92:95], v[108:111]\n"
      "v_exp_f32 v114, v114\n"
      "s_waitcnt lgkmcnt(4)\n"
      "v_mfma_f32_16x16x32_bf16 v[96:99], v[192:195], v[64:67], v[96:99]\n"
      "ds_read_b128 v[92:95], v5 offset:22528\n"
      "v_add_f32 v114, 1.0, v114\n"
      "v_mfma_f32_16x16x32_bf16 v[104:107], v[196:199], v[64:67], v[104:107]\n"
      "v_rcp_f32 v114, v114\n"
      "v_mfma_f32_16x16x32_bf16 v[100:103], v[192:195], v[68:71], v[100:103]\n"
      "ds_read_b128 v[64:67], v4 offset:24576\n"
      "v_mul_f32 v123, v115, v123\n"
      "v_mfma_f32_16x16x32_bf16 v[108:111], v[196:199], v[68:71], v[108:111]\n"
      "v_mul_f32 v114, v114, v122\n"
      "v_mul_f32 v115, s85, v115\n"
      "v_mfma_f32_16x16x32_bf16 v[96:99], v[200:203], v[72:75], v[96:99]\n"
      "ds_read_b128 v[68:71], v4 offset:26624\n"
      "s_waitcnt lgkmcnt(4)\n"
      "v_mfma_f32_16x16x32_bf16 v[104:107], v[204:207], v[72:75], v[104:107]\n"
      "v_exp_f32 v115, v115\n"
      "v_mfma_f32_16x16x32_bf16 v[100:103], v[200:203], v[76:79], v[100:103]\n"
      "ds_read_b128 v[72:75], v5 offset:24576\n"
      "v_add_f32 v115, 1.0, v115\n"
      "v_mfma_f32_16x16x32_bf16 v[108:111], v[204:207], v[76:79], v[108:111]\n"
      "v_rcp_f32 v115, v115\n"
      "v_mfma_f32_16x16x32_bf16 v[96:99], v[208:211], v[80:83], v[96:99]\n"
      "ds_read_b128 v[76:79], v5 offset:26624\n"
      "v_mul_f32 v126, v118, v126\n"
      "v_mfma_f32_16x16x32_bf16 v[104:107], v[212:215], v[80:83], v[104:107]\n"
      "v_mul_f32 v115, v115, v123\n"
      "v_mul_f32 v118, s85, v118\n"
      "v_mfma_f32_16x16x32_bf16 v[100:103], v[208:211], v[84:87], v[100:103]\n"
      "ds_read_b128 v[80:83], v4 offset:28672\n"
      "v_cvt_pk_bf16_f32 v17, v114, v115\n"
      "v_mfma_f32_16x16x32_bf16 v[108:111], v[212:215], v[84:87], v[108:111]\n"
      "v_exp_f32 v118, v118\n"
      "s_waitcnt lgkmcnt(4)\n"
      "v_mfma_f32_16x16x32_bf16 v[96:99], v[216:219], v[88:91], v[96:99]\n"
      "ds_read_b128 v[84:87], v4 offset:30720\n"
      "v_add_f32 v118, 1.0, v118\n"
      "v_mfma_f32_16x16x32_bf16 v[104:107], v[220:223], v[88:91], v[104:107]\n"
      "v_rcp_f32 v118, v118\n"
      "v_mfma_f32_16x16x32_bf16 v[100:103], v[216:219], v[92:95], v[100:103]\n"
      "ds_read_b128 v[88:91], v5 offset:28672\n"
      "v_mul_f32 v127, v119, v127\n"
      "v_mfma_f32_16x16x32_bf16 v[108:111], v[220:223], v[92:95], v[108:111]\n"
      "v_mul_f32 v118, v118, v126\n"
      "v_mul_f32 v119, s85, v119\n"
      "v_mfma_f32_16x16x32_bf16 v[96:99], v[224:227], v[64:67], v[96:99]\n"
      "ds_read_b128 v[92:95], v5 offset:30720\n"
      "s_waitcnt lgkmcnt(4)\n"
      "v_mfma_f32_16x16x32_bf16 v[104:107], v[228:231], v[64:67], v[104:107]\n"
      "v_exp_f32 v119, v119\n"
      "v_mfma_f32_16x16x32_bf16 v[100:103], v[224:227], v[68:71], v[100:103]\n"
      "v_add_f32 v119, 1.0, v119\n"
      "v_mfma_f32_16x16x32_bf16 v[108:111], v[228:231], v[68:71], v[108:111]\n"
      "v_rcp_f32 v119, v119\n"
      "v_mfma_f32_16x16x32_bf16 v[96:99], v[232:235], v[72:75], v[96:99]\n"
      "s_waitcnt lgkmcnt(0)\n"
      "s_barrier\n"
      "ds_read_b128 v[64:67], v4 offset:32768\n"
      "v_mul_f32 v119, v119, v127\n"
      "v_mfma_f32_16x16x32_bf16 v[104:107], v[236:239], v[72:75], v[104:107]\n"
      "ds_read_b128 v[68:71], v4 offset:34816\n"
      "v_cvt_pk_bf16_f32 v19, v118, v119\n"
      "v_mfma_f32_16x16x32_bf16 v[100:103], v[232:235], v[76:79], v[100:103]\n"
      "ds_read_b128 v[72:75], v5 offset:32768\n"
      "v_permlane16_swap_b32 v16, v18\n"
      "v_mfma_f32_16x16x32_bf16 v[108:111], v[236:239], v[76:79], v[108:111]\n"
      "v_permlane16_swap_b32 v17, v19\n"
      "v_mfma_f32_16x16x32_bf16 v[96:99], v[240:243], v[80:83], v[96:99]\n"
      "ds_read_b128 v[76:79], v5 offset:34816\n"
      "v_mfma_f32_16x16x32_bf16 v[104:107], v[244:247], v[80:83], v[104:107]\n"
      "buffer_store_dwordx4 v[16:19], v9, s[48:51], s25 offen sc1\n"
      "v_mfma_f32_16x16x32_bf16 v[100:103], v[240:243], v[84:87], v[100:103]\n"
      "ds_read_b128 v[80:83], v4 offset:36864\n"
      "v_mfma_f32_16x16x32_bf16 v[108:111], v[244:247], v[84:87], v[108:111]\n"
      "v_mfma_f32_16x16x32_bf16 v[96:99], v[248:251], v[88:91], v[96:99]\n"
      "ds_read_b128 v[84:87], v4 offset:38912\n"
      "s_waitcnt lgkmcnt(4)\n"
      "v_mfma_f32_16x16x32_bf16 v[104:107], v[252:255], v[88:91], v[104:107]\n"
      "v_mfma_f32_16x16x32_bf16 v[100:103], v[248:251], v[92:95], v[100:103]\n"
      "ds_read_b128 v[88:91], v5 offset:36864\n"
      "v_mfma_f32_16x16x32_bf16 v[108:111], v[252:255], v[92:95], v[108:111]\n"
      "v_mfma_f32_16x16x32_bf16 v[112:115], v[128:131], v[64:67], 0\n"
      "ds_read_b128 v[92:95], v5 offset:38912\n"
      "s_waitcnt lgkmcnt(4)\n"
      "v_mfma_f32_16x16x32_bf16 v[120:123], v[132:135], v[64:67], 0\n"
      "v_mfma_f32_16x16x32_bf16 v[116:119], v[128:131], v[68:71], 0\n"
      "ds_read_b128 v[64:67], v4 offset:40960\n"
      "v_mfma_f32_16x16x32_bf16 v[124:127], v[132:135], v[68:71], 0\n"
      "v_mfma_f32_16x16x32_bf16 v[112:115], v[136:139], v[72:75], v[112:115]\n"
      "ds_read_b128 v[68:71], v4 offset:43008\n"
      "v_mul_f32 v104, v96, v104\n"
      "v_mfma_f32_16x16x32_bf16 v[120:123], v[140:143], v[72:75], v[120:123]\n"
      "v_mul_f32 v96, s85, v96\n"
      "v_mfma_f32_16x16x32_bf16 v[116:119], v[136:139], v[76:79], v[116:119]\n"
      "ds_read_b128 v[72:75], v5 offset:40960\n"
      "s_waitcnt lgkmcnt(4)\n"
      "v_mfma_f32_16x16x32_bf16 v[124:127], v[140:143], v[76:79], v[124:127]\n"
      "v_exp_f32 v96, v96\n"
      "v_mfma_f32_16x16x32_bf16 v[112:115], v[144:147], v[80:83], v[112:115]\n"
      "ds_read_b128 v[76:79], v5 offset:43008\n"
      "v_add_f32 v96, 1.0, v96\n"
      "v_mfma_f32_16x16x32_bf16 v[120:123], v[148:151], v[80:83], v[120:123]\n"
      "v_rcp_f32 v96, v96\n"
      "v_mfma_f32_16x16x32_bf16 v[116:119], v[144:147], v[84:87], v[116:119]\n"
      "ds_read_b128 v[80:83], v4 offset:45056\n"
      "v_mul_f32 v105, v97, v105\n"
      "v_mfma_f32_16x16x32_bf16 v[124:127], v[148:151], v[84:87], v[124:127]\n"
      "v_mul_f32 v96, v96, v104\n"
      "v_mul_f32 v97, s85, v97\n"
      "v_mfma_f32_16x16x32_bf16 v[112:115], v[152:155], v[88:91], v[112:115]\n"
      "ds_read_b128 v[84:87], v4 offset:47104\n"
      "s_waitcnt lgkmcnt(4)\n"
      "v_mfma_f32_16x16x32_bf16 v[120:123], v[156:159], v[88:91], v[120:123]\n"
      "v_exp_f32 v97, v97\n"
      "v_mfma_f32_16x16x32_bf16 v[116:119], v[152:155], v[92:95], v[116:119]\n"
      "ds_read_b128 v[88:91], v5 offset:45056\n"
      "v_add_f32 v97, 1.0, v97\n"
      "v_mfma_f32_16x16x32_bf16 v[124:127], v[156:159], v[92:95], v[124:127]\n"
      "v_rcp_f32 v97, v97\n"
      "v_mfma_f32_16x16x32_bf16 v[112:115], v[160:163], v[64:67], v[112:115]\n"
      "ds_read_b128 v[92:95], v5 offset:47104\n"
      "v_mul_f32 v108, v100, v108\n"
      "v_mfma_f32_16x16x32_bf16 v[120:123], v[164:167], v[64:67], v[120:123]\n"
      "v_mul_f32 v97, v97, v105\n"
      "v_mul_f32 v100, s85, v100\n"
      "v_mfma_f32_16x16x32_bf16 v[116:119], v[160:163], v[68:71], v[116:119]\n"
      "v_cvt_pk_bf16_f32 v20, v96, v97\n"
      "s_waitcnt lgkmcnt(3)\n"
      "v_mfma_f32_16x16x32_bf16 v[124:127], v[164:167], v[68:71], v[124:127]\n"
      "v_exp_f32 v100, v100\n"
      "v_mfma_f32_16x16x32_bf16 v[112:115], v[168:171], v[72:75], v[112:115]\n"
      "s_waitcnt lgkmcnt(0)\n"
      "s_barrier\n"
      "ds_read_b128 v[64:67], v4 offset:49152\n"
      "v_add_f32 v100, 1.0, v100\n"
      "v_mfma_f32_16x16x32_bf16 v[120:123], v[172:175], v[72:75], v[120:123]\n"
      "ds_read_b128 v[68:71], v4 offset:51200\n"
      "v_rcp_f32 v100, v100\n"
      "v_mfma_f32_16x16x32_bf16 v[116:119], v[168:171], v[76:79], v[116:119]\n"
      "ds_read_b128 v[72:75], v5 offset:49152\n"
      "v_mul_f32 v109, v101, v109\n"
      "v_mfma_f32_16x16x32_bf16 v[124:127], v[172:175], v[76:79], v[124:127]\n"
      "v_mul_f32 v100, v100, v108\n"
      "v_mul_f32 v101, s85, v101\n"
      "v_mfma_f32_16x16x32_bf16 v[112:115], v[176:179], v[80:83], v[112:115]\n"
      "ds_read_b128 v[76:79], v5 offset:51200\n"
      "v_mfma_f32_16x16x32_bf16 v[120:123], v[180:183], v[80:83], v[120:123]\n"
      "v_exp_f32 v101, v101\n"
      "v_mfma_f32_16x16x32_bf16 v[116:119], v[176:179], v[84:87], v[116:119]\n"
      "ds_read_b128 v[80:83], v4 offset:53248\n"
      "v_add_f32 v101, 1.0, v101\n"
      "v_mfma_f32_16x16x32_bf16 v[124:127], v[180:183], v[84:87], v[124:127]\n"
      "v_rcp_f32 v101, v101\n"
      "v_mfma_f32_16x16x32_bf16 v[112:115], v[184:187], v[88:91], v[112:115]\n"
      "ds_read_b128 v[84:87], v4 offset:55296\n"
      "v_mul_f32 v106, v98, v106\n"
      "v_mfma_f32_16x16x32_bf16 v[120:123], v[188:191], v[88:91], v[120:123]\n"
      "v_mul_f32 v101, v101, v109\n"
      "v_mul_f32 v98, s85, v98\n"
      "v_mfma_f32_16x16x32_bf16 v[116:119], v[184:187], v[92:95], v[116:119]\n"
      "ds_read_b128 v[88:91], v5 offset:53248\n"
      "v_cvt_pk_bf16_f32 v22, v100, v101\n"
      "v_mfma_f32_16x16x32_bf16 v[124:127], v[188:191], v[92:95], v[124:127]\n"
      "v_exp_f32 v98, v98\n"
      "s_waitcnt lgkmcnt(4)\n"
      "v_mfma_f32_16x16x32_bf16 v[112:115], v[192:195], v[64:67], v[112:115]\n"
      "ds_read_b128 v[92:95], v5 offset:55296\n"
      "v_add_f32 v98, 1.0, v98\n"
      "v_mfma_f32_16x16x32_bf16 v[120:123], v[196:199], v[64:67], v[120:123]\n"
      "v_rcp_f32 v98, v98\n"
      "v_mfma_f32_16x16x32_bf16 v[116:119], v[192:195], v[68:71], v[116:119]\n"
      "ds_read_b128 v[64:67], v4 offset:57344\n"
      "v_mul_f32 v107, v99, v107\n"
      "v_mfma_f32_16x16x32_bf16 v[124:127], v[196:199], v[68:71], v[124:127]\n"
      "v_mul_f32 v98, v98, v106\n"
      "v_mul_f32 v99, s85, v99\n"
      "v_mfma_f32_16x16x32_bf16 v[112:115], v[200:203], v[72:75], v[112:115]\n"
      "ds_read_b128 v[68:71], v4 offset:59392\n"
      "s_waitcnt lgkmcnt(4)\n"
      "v_mfma_f32_16x16x32_bf16 v[120:123], v[204:207], v[72:75], v[120:123]\n"
      "v_exp_f32 v99, v99\n"
      "s_waitcnt vmcnt(0)\n"
      "buffer_store_dwordx2 v[12:13], v11, s[52:55], s29 offen sc1\n"
      "v_mfma_f32_16x16x32_bf16 v[116:119], v[200:203], v[76:79], v[116:119]\n"
      "ds_read_b128 v[72:75], v5 offset:57344\n"
      "v_add_f32 v99, 1.0, v99\n"
      "v_mfma_f32_16x16x32_bf16 v[124:127], v[204:207], v[76:79], v[124:127]\n"
      "v_rcp_f32 v99, v99\n"
      "v_mfma_f32_16x16x32_bf16 v[112:115], v[208:211], v[80:83], v[112:115]\n"
      "ds_read_b128 v[76:79], v5 offset:59392\n"
      "v_mul_f32 v110, v102, v110\n"
      "v_mfma_f32_16x16x32_bf16 v[120:123], v[212:215], v[80:83], v[120:123]\n"
      "v_mul_f32 v99, v99, v107\n"
      "v_mul_f32 v102, s85, v102\n"
      "v_mfma_f32_16x16x32_bf16 v[116:119], v[208:211], v[84:87], v[116:119]\n"
      "ds_read_b128 v[80:83], v4 offset:61440\n"
      "v_cvt_pk_bf16_f32 v21, v98, v99\n"
      "v_mfma_f32_16x16x32_bf16 v[124:127], v[212:215], v[84:87], v[124:127]\n"
      "v_exp_f32 v102, v102\n"
      "s_waitcnt lgkmcnt(4)\n"
      "v_mfma_f32_16x16x32_bf16 v[112:115], v[216:219], v[88:91], v[112:115]\n"
      "ds_read_b128 v[84:87], v4 offset:63488\n"
      "v_add_f32 v102, 1.0, v102\n"
      "v_mfma_f32_16x16x32_bf16 v[120:123], v[220:223], v[88:91], v[120:123]\n"
      "v_rcp_f32 v102, v102\n"
      "v_mfma_f32_16x16x32_bf16 v[116:119], v[216:219], v[92:95], v[116:119]\n"
      "ds_read_b128 v[88:91], v5 offset:61440\n"
      "v_mul_f32 v111, v103, v111\n"
      "v_mfma_f32_16x16x32_bf16 v[124:127], v[220:223], v[92:95], v[124:127]\n"
      "v_mul_f32 v102, v102, v110\n"
      "v_mul_f32 v103, s85, v103\n"
      "v_mfma_f32_16x16x32_bf16 v[112:115], v[224:227], v[64:67], v[112:115]\n"
      "ds_read_b128 v[92:95], v5 offset:63488\n"
      "s_waitcnt lgkmcnt(4)\n"
      "v_mfma_f32_16x16x32_bf16 v[120:123], v[228:231], v[64:67], v[120:123]\n"
      "v_exp_f32 v103, v103\n"
      "v_mfma_f32_16x16x32_bf16 v[116:119], v[224:227], v[68:71], v[116:119]\n"
      "v_add_f32 v103, 1.0, v103\n"
      "v_mfma_f32_16x16x32_bf16 v[124:127], v[228:231], v[68:71], v[124:127]\n"
      "v_rcp_f32 v103, v103\n"
      "v_mfma_f32_16x16x32_bf16 v[112:115], v[232:235], v[72:75], v[112:115]\n"
      "s_waitcnt lgkmcnt(0)\n"
      "s_barrier\n"
      "ds_read_b64_tr_b16 v[32:33], v24 offset:0\n"
      "ds_read_b64_tr_b16 v[34:35], v24 offset:512\n"
      "v_mul_f32 v103, v103, v111\n"
      "v_mfma_f32_16x16x32_bf16 v[120:123], v[236:239], v[72:75], v[120:123]\n"
      "ds_read_b64_tr_b16 v[36:37], v25 offset:0\n"
      "ds_read_b64_tr_b16 v[38:39], v25 offset:512\n"
      "v_cvt_pk_bf16_f32 v23, v102, v103\n"
      "v_mfma_f32_16x16x32_bf16 v[116:119], v[232:235], v[76:79], v[116:119]\n"
      "ds_read_b64_tr_b16 v[40:41], v26 offset:0\n"
      "ds_read_b64_tr_b16 v[42:43], v26 offset:512\n"
      "v_permlane16_swap_b32 v20, v22\n"
      "v_mfma_f32_16x16x32_bf16 v[124:127], v[236:239], v[76:79], v[124:127]\n"
      "ds_read_b64_tr_b16 v[44:45], v27 offset:0\n"
      "ds_read_b64_tr_b16 v[46:47], v27 offset:512\n"
      "v_permlane16_swap_b32 v21, v23\n"
      "buffer_load_dwordx4 v[128:131], v10, s[36:39], s81 offen offset:0 sc1\n"
      "v_mfma_f32_16x16x32_bf16 v[112:115], v[240:243], v[80:83], v[112:115]\n"
      "ds_read_b64_tr_b16 v[48:49], v24 offset:8192\n"
      "ds_read_b64_tr_b16 v[50:51], v24 offset:8704\n"
      "v_mfma_f32_16x16x32_bf16 v[120:123], v[244:247], v[80:83], v[120:123]\n"
      "ds_read_b64_tr_b16 v[52:53], v25 offset:8192\n"
      "ds_read_b64_tr_b16 v[54:55], v25 offset:8704\n"
      "v_mfma_f32_16x16x32_bf16 v[116:119], v[240:243], v[84:87], v[116:119]\n"
      "ds_read_b64_tr_b16 v[56:57], v26 offset:8192\n"
      "ds_read_b64_tr_b16 v[58:59], v26 offset:8704\n"
      "v_mfma_f32_16x16x32_bf16 v[124:127], v[244:247], v[84:87], v[124:127]\n"
      "ds_read_b64_tr_b16 v[60:61], v27 offset:8192\n"
      "ds_read_b64_tr_b16 v[62:63], v27 offset:8704\n"
      "buffer_load_dwordx4 v[132:135], v30, s[36:39], s81 offen offset:0 sc1\n"
      "v_mfma_f32_16x16x32_bf16 v[112:115], v[248:251], v[88:91], v[112:115]\n"
      "v_mfma_f32_16x16x32_bf16 v[120:123], v[252:255], v[88:91], v[120:123]\n"
      "v_mfma_f32_16x16x32_bf16 v[116:119], v[248:251], v[92:95], v[116:119]\n"
      "v_mfma_f32_16x16x32_bf16 v[124:127], v[252:255], v[92:95], v[124:127]\n"
      "buffer_load_dwordx4 v[136:139], v10, s[36:39], s81 offen offset:2048 sc1\n"
      "s_nop 5\n"
      "v_mul_f32 v120, v112, v120\n"
      "v_mul_f32 v121, v113, v121\n"
      "v_mul_f32 v124, v116, v124\n"
      "v_mul_f32 v125, v117, v125\n"
      "v_mul_f32 v112, s85, v112\n"
      "v_mul_f32 v113, s85, v113\n"
      "v_mul_f32 v116, s85, v116\n"
      "v_mul_f32 v117, s85, v117\n"
      "v_exp_f32 v112, v112\n"
      "v_exp_f32 v113, v113\n"
      "v_exp_f32 v116, v116\n"
      "v_exp_f32 v117, v117\n"
      "v_add_f32 v112, 1.0, v112\n"
      "v_add_f32 v113, 1.0, v113\n"
      "v_add_f32 v116, 1.0, v116\n"
      "v_add_f32 v117, 1.0, v117\n"
      "v_rcp_f32 v112, v112\n"
      "v_rcp_f32 v113, v113\n"
      "v_rcp_f32 v116, v116\n"
      "buffer_load_dwordx4 v[140:143], v30, s[36:39], s81 offen offset:2048 sc1\n"
      "v_rcp_f32 v117, v117\n"
      "v_mul_f32 v112, v112, v120\n"
      "v_mul_f32 v113, v113, v121\n"
      "v_mul_f32 v116, v116, v124\n"
      "v_mul_f32 v117, v117, v125\n"
      "v_cvt_pk_bf16_f32 v16, v112, v113\n"
      "v_cvt_pk_bf16_f32 v18, v116, v117\n"
      "v_mul_f32 v122, v114, v122\n"
      "v_mul_f32 v123, v115, v123\n"
      "v_mul_f32 v126, v118, v126\n"
      "v_mul_f32 v127, v119, v127\n"
      "v_permlane16_swap_b32 v16, v18\n"
      "v_mul_f32 v114, s85, v114\n"
      "v_mul_f32 v115, s85, v115\n"
      "v_mul_f32 v118, s85, v118\n"
      "v_mul_f32 v119, s85, v119\n"
      "v_exp_f32 v114, v114\n"
      "v_exp_f32 v115, v115\n"
      "v_exp_f32 v118, v118\n"
      "s_add_u32 s81, s81, 4096\n"
      "buffer_load_dwordx4 v[144:147], v10, s[36:39], s81 offen offset:0 sc1\n"
      "v_exp_f32 v119, v119\n"
      "v_add_f32 v114, 1.0, v114\n"
      "v_add_f32 v115, 1.0, v115\n"
      "v_add_f32 v118, 1.0, v118\n"
      "v_add_f32 v119, 1.0, v119\n"
      "v_rcp_f32 v114, v114\n"
      "v_rcp_f32 v115, v115\n"
      "v_rcp_f32 v118, v118\n"
      "v_rcp_f32 v119, v119\n"
      "v_mul_f32 v114, v114, v122\n"
      "v_mul_f32 v115, v115, v123\n"
      "v_mul_f32 v118, v118, v126\n"
      "v_mul_f32 v119, v119, v127\n"
      "v_cvt_pk_bf16_f32 v17, v114, v115\n"
      "v_cvt_pk_bf16_f32 v19, v118, v119\n"
      "s_nop 1\n"
      "v_permlane16_swap_b32 v17, v19\n"
      "buffer_store_dwordx4 v[20:23], v8, s[48:51], s26 offen sc1\n"
      "buffer_store_dwordx4 v[16:19], v9, s[48:51], s26 offen sc1\n"
      "buffer_load_dwordx4 v[148:151], v30, s[36:39], s81 offen offset:0 sc1\n"
      "buffer_load_dwordx4 v[152:155], v10, s[36:39], s81 offen offset:2048 sc1\n"
      "buffer_load_dwordx4 v[156:159], v30, s[36:39], s81 offen offset:2048 sc1\n"
      "s_add_u32 s81, s81, 4096\n"
      "buffer_load_dwordx4 v[160:163], v10, s[36:39], s81 offen offset:0 sc1\n"
      "buffer_load_dwordx4 v[164:167], v30, s[36:39], s81 offen offset:0 sc1\n"
      "buffer_load_dwordx4 v[168:171], v10, s[36:39], s81 offen offset:2048 sc1\n"
      "s_waitcnt vmcnt(11)\n"
      "s_waitcnt lgkmcnt(12)\n"
      "v_mfma_f32_16x16x32_bf16 v[96:99], v[32:35], v[128:131], 0\n"
      "buffer_load_dwordx4 v[172:175], v30, s[36:39], s81 offen offset:2048 sc1\n"
      "v_mfma_f32_16x16x32_bf16 v[64:67], v[32:35], v[132:135], 0\n"
      "ds_read_b64_tr_b16 v[32:33], v24 offset:16384\n"
      "ds_read_b64_tr_b16 v[34:35], v24 offset:16896\n"
      "v_mfma_f32_16x16x32_bf16 v[100:103], v[36:39], v[128:131], 0\n"
      "v_mfma_f32_16x16x32_bf16 v[68:71], v[36:39], v[132:135], 0\n"
      "ds_read_b64_tr_b16 v[36:37], v25 offset:16384\n"
      "ds_read_b64_tr_b16 v[38:39], v25 offset:16896\n"
      "s_waitcnt lgkmcnt(12)\n"
      "v_mfma_f32_16x16x32_bf16 v[104:107], v[40:43], v[128:131], 0\n"
      "s_add_u32 s81, s81, 4096\n"
      "buffer_load_dwordx4 v[176:179], v10, s[36:39], s81 offen offset:0 sc1\n"
      "v_mfma_f32_16x16x32_bf16 v[72:75], v[40:43], v[132:135], 0\n"
      "ds_read_b64_tr_b16 v[40:41], v26 offset:16384\n"
      "ds_read_b64_tr_b16 v[42:43], v26 offset:16896\n"
      "v_mfma_f32_16x16x32_bf16 v[108:111], v[44:47], v[128:131], 0\n"
      "v_mfma_f32_16x16x32_bf16 v[76:79], v[44:47], v[132:135], 0\n"
      "ds_read_b64_tr_b16 v[44:45], v27 offset:16384\n"
      "ds_read_b64_tr_b16 v[46:47], v27 offset:16896\n"
      "s_waitcnt vmcnt(11)\n"
      "s_waitcnt lgkmcnt(12)\n"
      "v_mfma_f32_16x16x32_bf16 v[96:99], v[48:51], v[136:139], v[96:99]\n"
      "buffer_load_dwordx4 v[180:183], v30, s[36:39], s81 offen offset:0 sc1\n"
      "v_mfma_f32_16x16x32_bf16 v[64:67], v[48:51], v[140:143], v[64:67]\n"
      "ds_read_b64_tr_b16 v[48:49], v24 offset:24576\n"
      "ds_read_b64_tr_b16 v[50:51], v24 offset:25088\n"
      "v_mfma_f32_16x16x32_bf16 v[100:103], v[52:55], v[136:139], v[100:103]\n"
      "s_add_u32 m0, s21, 122880\n"
      "s_nop 0\n"
      "buffer_load_dwordx4 v14, s[32:35], s20 offen lds\n"
      "v_mfma_f32_16x16x32_bf16 v[68:71], v[52:55], v[140:143], v[68:71]\n"
      "ds_read_b64_tr_b16 v[52:53], v25 offset:24576\n"
      "ds_read_b64_tr_b16 v[54:55], v25 offset:25088\n"
      "s_waitcnt lgkmcnt(12)\n"
      "v_mfma_f32_16x16x32_bf16 v[104:107], v[56:59], v[136:139], v[104:107]\n"
      "s_add_u32 m0, s21, 126976\n"
      "s_add_u32 s20, s20, 32768\n"
      "buffer_load_dwordx4 v14, s[32:35], s20 offen lds\n"
      "v_mfma_f32_16x16x32_bf16 v[72:75], v[56:59], v[140:143], v[72:75]\n"
      "ds_read_b64_tr_b16 v[56:57], v26 offset:24576\n"
      "ds_read_b64_tr_b16 v[58:59], v26 offset:25088\n"
      "v_mfma_f32_16x16x32_bf16 v[108:111], v[60:63], v[136:139], v[108:111]\n"
      "v_mfma_f32_16x16x32_bf16 v[76:79], v[60:63], v[140:143], v[76:79]\n"
      "ds_read_b64_tr_b16 v[60:61], v27 offset:24576\n"
      "ds_read_b64_tr_b16 v[62:63], v27 offset:25088\n"
      "s_waitcnt vmcnt(11)\n"
      "buffer_store_dwordx2 v[12:13], v11, s[52:55], s30 offen sc1\n"
      "s_waitcnt vmcnt(11)\n"
      "s_waitcnt lgkmcnt(12)\n"
      "v_mfma_f32_16x16x32_bf16 v[96:99], v[32:35], v[144:147], v[96:99]\n"
      "v_mfma_f32_16x16x32_bf16 v[64:67], v[32:35], v[148:151], v[64:67]\n"
      "ds_read_b64_tr_b16 v[32:33], v24 offset:32768\n"
      "ds_read_b64_tr_b16 v[34:35], v24 offset:33280\n"
      "v_mfma_f32_16x16x32_bf16 v[100:103], v[36:39], v[144:147], v[100:103]\n"
      "v_mfma_f32_16x16x32_bf16 v[68:71], v[36:39], v[148:151], v[68:71]\n"
      "ds_read_b64_tr_b16 v[36:37], v25 offset:32768\n"
      "ds_read_b64_tr_b16 v[38:39], v25 offset:33280\n"
      "s_waitcnt lgkmcnt(12)\n"
      "v_mfma_f32_16x16x32_bf16 v[104:107], v[40:43], v[144:147], v[104:107]\n"
      "v_mfma_f32_16x16x32_bf16 v[72:75], v[40:43], v[148:151], v[72:75]\n"
      "ds_read_b64_tr_b16 v[40:41], v26 offset:32768\n"
      "ds_read_b64_tr_b16 v[42:43], v26 offset:33280\n"
      "v_mfma_f32_16x16x32_bf16 v[108:111], v[44:47], v[144:147], v[108:111]\n"
      "v_mfma_f32_16x16x32_bf16 v[76:79], v[44:47], v[148:151], v[76:79]\n"
      "ds_read_b64_tr_b16 v[44:45], v27 offset:32768\n"
      "ds_read_b64_tr_b16 v[46:47], v27 offset:33280\n"
      "s_waitcnt vmcnt(9)\n"
      "s_waitcnt lgkmcnt(12)\n"
      "v_mfma_f32_16x16x32_bf16 v[96:99], v[48:51], v[152:155], v[96:99]\n"
      "s_barrier\n"
      "buffer_load_dwordx4 v[184:187], v10, s[36:39], s82 offen offset:0 sc1\n"
      "v_mfma_f32_16x16x32_bf16 v[64:67], v[48:51], v[156:159], v[64:67]\n"
      "ds_read_b64_tr_b16 v[48:49], v24 offset:40960\n"
      "ds_read_b64_tr_b16 v[50:51], v24 offset:41472\n"
      "v_mfma_f32_16x16x32_bf16 v[100:103], v[52:55], v[152:155], v[100:103]\n"
      "buffer_load_dwordx4 v[188:191], v30, s[36:39], s82 offen offset:0 sc1\n"
      "v_mfma_f32_16x16x32_bf16 v[68:71], v[52:55], v[156:159], v[68:71]\n"
      "ds_read_b64_tr_b16 v[52:53], v25 offset:40960\n"
      "ds_read_b64_tr_b16 v[54:55], v25 offset:41472\n"
      "s_waitcnt lgkmcnt(12)\n"
      "v_mfma_f32_16x16x32_bf16 v[104:107], v[56:59], v[152:155], v[104:107]\n"
      "buffer_load_dwordx4 v[192:195], v10, s[36:39], s83 offen offset:0 sc1\n"
      "v_mfma_f32_16x16x32_bf16 v[72:75], v[56:59], v[156:159], v[72:75]\n"
      "ds_read_b64_tr_b16 v[56:57], v26 offset:40960\n"
      "ds_read_b64_tr_b16 v[58:59], v26 offset:41472\n"
      "v_mfma_f32_16x16x32_bf16 v[108:111], v[60:63], v[152:155], v[108:111]\n"
      "buffer_load_dwordx4 v[196:199], v30, s[36:39], s83 offen offset:0 sc1\n"
      "v_mfma_f32_16x16x32_bf16 v[76:79], v[60:63], v[156:159], v[76:79]\n"
      "ds_read_b64_tr_b16 v[60:61], v27 offset:40960\n"
      "ds_read_b64_tr_b16 v[62:63], v27 offset:41472\n"
      "s_waitcnt vmcnt(11)\n"
      "s_waitcnt lgkmcnt(12)\n"
      "v_mfma_f32_16x16x32_bf16 v[96:99], v[32:35], v[160:163], v[96:99]\n"
      "s_add_u32 m0, s21, 131072\n"
      "s_add_u32 s20, s20, 32768\n"
      "buffer_load_dwordx4 v14, s[32:35], s20 offen lds\n"
      "v_mfma_f32_16x16x32_bf16 v[64:67], v[32:35], v[164:167], v[64:67]\n"
      "ds_read_b64_tr_b16 v[32:33], v24 offset:49152\n"
      "ds_read_b64_tr_b16 v[34:35], v24 offset:49664\n"
      "v_mfma_f32_16x16x32_bf16 v[100:103], v[36:39], v[160:163], v[100:103]\n"
      "v_mfma_f32_16x16x32_bf16 v[68:71], v[36:39], v[164:167], v[68:71]\n"
      "ds_read_b64_tr_b16 v[36:37], v25 offset:49152\n"
      "ds_read_b64_tr_b16 v[38:39], v25 offset:49664\n"
      "s_waitcnt lgkmcnt(12)\n"
      "v_mfma_f32_16x16x32_bf16 v[104:107], v[40:43], v[160:163], v[104:107]\n"
      "buffer_load_dwordx4 v[200:203], v10, s[36:39], s83 offen offset:2048 sc1\n"
      "v_mfma_f32_16x16x32_bf16 v[72:75], v[40:43], v[164:167], v[72:75]\n"
      "ds_read_b64_tr_b16 v[40:41], v26 offset:49152\n"
      "ds_read_b64_tr_b16 v[42:43], v26 offset:49664\n"
      "v_mfma_f32_16x16x32_bf16 v[108:111], v[44:47], v[160:163], v[108:111]\n"
      "v_mfma_f32_16x16x32_bf16 v[76:79], v[44:47], v[164:167], v[76:79]\n"
      "ds_read_b64_tr_b16 v[44:45], v27 offset:49152\n"
      "ds_read_b64_tr_b16 v[46:47], v27 offset:49664\n"
      "s_waitcnt vmcnt(11)\n"
      "s_waitcnt lgkmcnt(12)\n"
      "v_mfma_f32_16x16x32_bf16 v[96:99], v[48:51], v[168:171], v[96:99]\n"
      "s_add_u32 m0, s21, 135168\n"
      "s_add_u32 s20, s20, 32768\n"
      "buffer_load_dwordx4 v14, s[32:35], s20 offen lds\n"
      "v_mfma_f32_16x16x32_bf16 v[64:67], v[48:51], v[172:175], v[64:67]\n"
      "ds_read_b64_tr_b16 v[48:49], v24 offset:57344\n"
      "ds_read_b64_tr_b16 v[50:51], v24 offset:57856\n"
      "v_mfma_f32_16x16x32_bf16 v[100:103], v[52:55], v[168:171], v[100:103]\n"
      "v_mfma_f32_16x16x32_bf16 v[68:71], v[52:55], v[172:175], v[68:71]\n"
      "ds_read_b64_tr_b16 v[52:53], v25 offset:57344\n"
      "ds_read_b64_tr_b16 v[54:55], v25 offset:57856\n"
      "s_waitcnt lgkmcnt(12)\n"
      "v_mfma_f32_16x16x32_bf16 v[104:107], v[56:59], v[168:171], v[104:107]\n"
      "buffer_load_dwordx4 v[204:207], v30, s[36:39], s83 offen offset:2048 sc1\n"
      "v_mfma_f32_16x16x32_bf16 v[72:75], v[56:59], v[172:175], v[72:75]\n"
      "ds_read_b64_tr_b16 v[56:57], v26 offset:57344\n"
      "ds_read_b64_tr_b16 v[58:59], v26 offset:57856\n"
      "v_mfma_f32_16x16x32_bf16 v[108:111], v[60:63], v[168:171], v[108:111]\n"
      "v_mfma_f32_16x16x32_bf16 v[76:79], v[60:63], v[172:175], v[76:79]\n"
      "ds_read_b64_tr_b16 v[60:61], v27 offset:57344\n"
      "ds_read_b64_tr_b16 v[62:63], v27 offset:57856\n"
      "s_waitcnt vmcnt(11)\n"
      "s_waitcnt lgkmcnt(12)\n"
      "v_mfma_f32_16x16x32_bf16 v[96:99], v[32:35], v[176:179], v[96:99]\n"
      "s_add_u32 s83, s83, 4096\n"
      "buffer_load_dwordx4 v[208:211], v10, s[36:39], s83 offen offset:0 sc1\n"
      "v_mfma_f32_16x16x32_bf16 v[64:67], v[32:35], v[180:183], v[64:67]\n"
      "v_add_u32 v24, 65536, v24\n"
      "v_add_u32 v25, 65536, v25\n"
      "v_add_u32 v26, 65536, v26\n"
      "v_add_u32 v27, 65536, v27\n"
      "ds_read_b64_tr_b16 v[32:33], v24 offset:0\n"
      "ds_read_b64_tr_b16 v[34:35], v24 offset:512\n"
      "v_mfma_f32_16x16x32_bf16 v[100:103], v[36:39], v[176:179], v[100:103]\n"
      "buffer_load_dwordx4 v[212:215], v30, s[36:39], s83 offen offset:0 sc1\n"
      "v_mfma_f32_16x16x32_bf16 v[68:71], v[36:39], v[180:183], v[68:71]\n"
      "ds_read_b64_tr_b16 v[36:37], v25 offset:0\n"
      "ds_read_b64_tr_b16 v[38:39], v25 offset:512\n"
      "s_waitcnt lgkmcnt(12)\n"
      "v_mfma_f32_16x16x32_bf16 v[104:107], v[40:43], v[176:179], v[104:107]\n"
      "buffer_load_dwordx4 v[216:219], v10, s[36:39], s83 offen offset:2048 sc1\n"
      "v_mfma_f32_16x16x32_bf16 v[72:75], v[40:43], v[180:183], v[72:75]\n"
      "ds_read_b64_tr_b16 v[40:41], v26 offset:0\n"
      "ds_read_b64_tr_b16 v[42:43], v26 offset:512\n"
      "v_mfma_f32_16x16x32_bf16 v[108:111], v[44:47], v[176:179], v[108:111]\n"
      "buffer_load_dwordx4 v[220:223], v30, s[36:39], s83 offen offset:2048 sc1\n"
      "s_add_u32 s83, s83, 4096\n"
      "buffer_load_dwordx4 v[224:227], v10, s[36:39], s83 offen offset:0 sc1\n"
      "v_mfma_f32_16x16x32_bf16 v[76:79], v[44:47], v[180:183], v[76:79]\n"
      "ds_read_b64_tr_b16 v[44:45], v27 offset:0\n"
      "ds_read_b64_tr_b16 v[46:47], v27 offset:512\n"
      "s_waitcnt vmcnt(11)\n"
      "s_waitcnt lgkmcnt(12)\n"
      "v_mfma_f32_16x16x32_bf16 v[96:99], v[48:51], v[184:187], v[96:99]\n"
      "buffer_load_dwordx4 v[228:231], v30, s[36:39], s83 offen offset:0 sc1\n"
      "v_mfma_f32_16x16x32_bf16 v[64:67], v[48:51], v[188:191], v[64:67]\n"
      "ds_read_b64_tr_b16 v[48:49], v24 offset:8192\n"
      "ds_read_b64_tr_b16 v[50:51], v24 offset:8704\n"
      "v_mfma_f32_16x16x32_bf16 v[100:103], v[52:55], v[184:187], v[100:103]\n"
      "v_mfma_f32_16x16x32_bf16 v[68:71], v[52:55], v[188:191], v[68:71]\n"
      "ds_read_b64_tr_b16 v[52:53], v25 offset:8192\n"
      "ds_read_b64_tr_b16 v[54:55], v25 offset:8704\n"
      "s_waitcnt lgkmcnt(12)\n"
      "v_mfma_f32_16x16x32_bf16 v[104:107], v[56:59], v[184:187], v[104:107]\n"
      "buffer_load_dwordx4 v[232:235], v10, s[36:39], s83 offen offset:2048 sc1\n"
      "v_mfma_f32_16x16x32_bf16 v[72:75], v[56:59], v[188:191], v[72:75]\n"
      "ds_read_b64_tr_b16 v[56:57], v26 offset:8192\n"
      "ds_read_b64_tr_b16 v[58:59], v26 offset:8704\n"
      "v_mfma_f32_16x16x32_bf16 v[108:111], v[60:63], v[184:187], v[108:111]\n"
      "v_mfma_f32_16x16x32_bf16 v[76:79], v[60:63], v[188:191], v[76:79]\n"
      "ds_read_b64_tr_b16 v[60:61], v27 offset:8192\n"
      "ds_read_b64_tr_b16 v[62:63], v27 offset:8704\n"
      "s_waitcnt vmcnt(11)\n"
      "s_waitcnt lgkmcnt(12)\n"
      "v_mfma_f32_16x16x32_bf16 v[96:99], v[32:35], v[192:195], v[96:99]\n"
      "buffer_load_dwordx4 v[236:239], v30, s[36:39], s83 offen offset:2048 sc1\n"
      "v_mfma_f32_16x16x32_bf16 v[64:67], v[32:35], v[196:199], v[64:67]\n"
      "ds_read_b64_tr_b16 v[32:33], v24 offset:16384\n"
      "ds_read_b64_tr_b16 v[34:35], v24 offset:16896\n"
      "v_mfma_f32_16x16x32_bf16 v[100:103], v[36:39], v[192:195], v[100:103]\n"
      "s_add_u32 m0, s21, 139264\n"
      "s_add_u32 s20, s20, 32768\n"
      "buffer_load_dwordx4 v14, s[32:35], s20 offen lds\n"
      "v_mfma_f32_16x16x32_bf16 v[68:71], v[36:39], v[196:199], v[68:71]\n"
      "ds_read_b64_tr_b16 v[36:37], v25 offset:16384\n"
      "ds_read_b64_tr_b16 v[38:39], v25 offset:16896\n"
      "s_waitcnt lgkmcnt(12)\n"
      "v_mfma_f32_16x16x32_bf16 v[104:107], v[40:43], v[192:195], v[104:107]\n"
      "s_add_u32 m0, s21, 143360\n"
      "s_add_u32 s20, s20, 32768\n"
      "buffer_load_dwordx4 v14, s[32:35], s20 offen lds\n"
      "v_mfma_f32_16x16x32_bf16 v[72:75], v[40:43], v[196:199], v[72:75]\n"
      "ds_read_b64_tr_b16 v[40:41], v26 offset:16384\n"
      "ds_read_b64_tr_b16 v[42:43], v26 offset:16896\n"
      "v_mfma_f32_16x16x32_bf16 v[108:111], v[44:47], v[192:195], v[108:111]\n"
      "s_add_u32 m0, s21, 147456\n"
      "s_add_u32 s20, s20, 32768\n"
      "buffer_load_dwordx4 v14, s[32:35], s20 offen lds\n"
      "v_mfma_f32_16x16x32_bf16 v[76:79], v[44:47], v[196:199], v[76:79]\n"
      "ds_read_b64_tr_b16 v[44:45], v27 offset:16384\n"
      "ds_read_b64_tr_b16 v[46:47], v27 offset:16896\n"
      "s_waitcnt vmcnt(11)\n"
      "s_waitcnt lgkmcnt(12)\n"
      "v_mfma_f32_16x16x32_bf16 v[96:99], v[48:51], v[200:203], v[96:99]\n"
      "s_add_u32 m0, s21, 151552\n"
      "s_add_u32 s20, s20, 32768\n"
      "buffer_load_dwordx4 v14, s[32:35], s20 offen lds\n"
      "v_mfma_f32_16x16x32_bf16 v[64:67], v[48:51], v[204:207], v[64:67]\n"
      "ds_read_b64_tr_b16 v[48:49], v24 offset:24576\n"
      "ds_read_b64_tr_b16 v[50:51], v24 offset:25088\n"
      "v_mfma_f32_16x16x32_bf16 v[100:103], v[52:55], v[200:203], v[100:103]\n"
      "v_mfma_f32_16x16x32_bf16 v[68:71], v[52:55], v[204:207], v[68:71]\n"
      "ds_read_b64_tr_b16 v[52:53], v25 offset:24576\n"
      "ds_read_b64_tr_b16 v[54:55], v25 offset:25088\n"
      "s_waitcnt lgkmcnt(12)\n"
      "v_mfma_f32_16x16x32_bf16 v[104:107], v[56:59], v[200:203], v[104:107]\n"
      "s_add_u32 m0, s21, 155648\n"
      "s_add_u32 s20, s20, 32768\n"
      "buffer_load_dwordx4 v14, s[32:35], s20 offen lds\n"
      "v_mfma_f32_16x16x32_bf16 v[72:75], v[56:59], v[204:207], v[72:75]\n"
      "ds_read_b64_tr_b16 v[56:57], v26 offset:24576\n"
      "ds_read_b64_tr_b16 v[58:59], v26 offset:25088\n"
      "v_mfma_f32_16x16x32_bf16 v[108:111], v[60:63], v[200:203], v[108:111]\n"
      "v_mfma_f32_16x16x32_bf16 v[76:79], v[60:63], v[204:207], v[76:79]\n"
      "ds_read_b64_tr_b16 v[60:61], v27 offset:24576\n"
      "ds_read_b64_tr_b16 v[62:63], v27 offset:25088\n"
      "s_waitcnt vmcnt(11)\n"
      "s_waitcnt lgkmcnt(12)\n"
      "v_mfma_f32_16x16x32_bf16 v[96:99], v[32:35], v[208:211], v[96:99]\n"
      "s_add_u32 m0, s21, 159744\n"
      "s_add_u32 s20, s20, 32768\n"
      "buffer_load_dwordx4 v14, s[32:35], s20 offen lds\n"
      "v_mfma_f32_16x16x32_bf16 v[64:67], v[32:35], v[212:215], v[64:67]\n"
      "ds_read_b64_tr_b16 v[32:33], v24 offset:32768\n"
      "ds_read_b64_tr_b16 v[34:35], v24 offset:33280\n"
      "v_mfma_f32_16x16x32_bf16 v[100:103], v[36:39], v[208:211], v[100:103]\n"
      "v_mfma_f32_16x16x32_bf16 v[68:71], v[36:39], v[212:215], v[68:71]\n"
      "ds_read_b64_tr_b16 v[36:37], v25 offset:32768\n"
      "ds_read_b64_tr_b16 v[38:39], v25 offset:33280\n"
      "s_waitcnt lgkmcnt(12)\n"
      "v_mfma_f32_16x16x32_bf16 v[104:107], v[40:43], v[208:211], v[104:107]\n"
      "s_add_u32 m0, s21, 0\n"
      "s_add_u32 s20, s20, 32768\n"
      "buffer_load_dwordx4 v14, s[32:35], s20 offen lds\n"
      "v_mfma_f32_16x16x32_bf16 v[72:75], v[40:43], v[212:215], v[72:75]\n"
      "ds_read_b64_tr_b16 v[40:41], v26 offset:32768\n"
      "ds_read_b64_tr_b16 v[42:43], v26 offset:33280\n"
      "v_mfma_f32_16x16x32_bf16 v[108:111], v[44:47], v[208:211], v[108:111]\n"
      "v_mfma_f32_16x16x32_bf16 v[76:79], v[44:47], v[212:215], v[76:79]\n"
      "ds_read_b64_tr_b16 v[44:45], v27 offset:32768\n"
      "ds_read_b64_tr_b16 v[46:47], v27 offset:33280\n"
      "s_waitcnt vmcnt(11)\n"
      "s_waitcnt lgkmcnt(12)\n"
      "v_mfma_f32_16x16x32_bf16 v[96:99], v[48:51], v[216:219], v[96:99]\n"
      "s_add_u32 m0, s21, 4096\n"
      "s_add_u32 s20, s20, 32768\n"
      "buffer_load_dwordx4 v14, s[32:35], s20 offen lds\n"
      "v_mfma_f32_16x16x32_bf16 v[64:67], v[48:51], v[220:223], v[64:67]\n"
      "ds_read_b64_tr_b16 v[48:49], v24 offset:40960\n"
      "ds_read_b64_tr_b16 v[50:51], v24 offset:41472\n"
      "v_mfma_f32_16x16x32_bf16 v[100:103], v[52:55], v[216:219], v[100:103]\n"
      "v_mfma_f32_16x16x32_bf16 v[68:71], v[52:55], v[220:223], v[68:71]\n"
      "ds_read_b64_tr_b16 v[52:53], v25 offset:40960\n"
      "ds_read_b64_tr_b16 v[54:55], v25 offset:41472\n"
      "s_waitcnt lgkmcnt(12)\n"
      "v_mfma_f32_16x16x32_bf16 v[104:107], v[56:59], v[216:219], v[104:107]\n"
      "s_add_u32 m0, s21, 8192\n"
      "s_add_u32 s20, s20, 32768\n"
      "buffer_load_dwordx4 v14, s[32:35], s20 offen lds\n"
      "v_mfma_f32_16x16x32_bf16 v[72:75], v[56:59], v[220:223], v[72:75]\n"
      "ds_read_b64_tr_b16 v[56:57], v26 offset:40960\n"
      "ds_read_b64_tr_b16 v[58:59], v26 offset:41472\n"
      "v_mfma_f32_16x16x32_bf16 v[108:111], v[60:63], v[216:219], v[108:111]\n"
      "v_mfma_f32_16x16x32_bf16 v[76:79], v[60:63], v[220:223], v[76:79]\n"
      "ds_read_b64_tr_b16 v[60:61], v27 offset:40960\n"
      "ds_read_b64_tr_b16 v[62:63], v27 offset:41472\n"
      "s_waitcnt vmcnt(11)\n"
      "s_waitcnt lgkmcnt(12)\n"
      "v_mfma_f32_16x16x32_bf16 v[96:99], v[32:35], v[224:227], v[96:99]\n"
      "s_barrier\n"
      "buffer_load_dwordx4 v[240:243], v10, s[36:39], s23 offen offset:0 sc1\n"
      "v_mfma_f32_16x16x32_bf16 v[64:67], v[32:35], v[228:231], v[64:67]\n"
      "v_subrev_u32 v24, s22, v24\n"
      "v_subrev_u32 v25, s22, v25\n"
      "v_subrev_u32 v26, s22, v26\n"
      "v_subrev_u32 v27, s22, v27\n"
      "ds_read_b64_tr_b16 v[32:33], v24 offset:49152\n"
      "ds_read_b64_tr_b16 v[34:35], v24 offset:49664\n"
      "v_mfma_f32_16x16x32_bf16 v[100:103], v[36:39], v[224:227], v[100:103]\n"
      "v_mfma_f32_16x16x32_bf16 v[68:71], v[36:39], v[228:231], v[68:71]\n"
      "ds_read_b64_tr_b16 v[36:37], v25 offset:49152\n"
      "ds_read_b64_tr_b16 v[38:39], v25 offset:49664\n"
      "s_waitcnt lgkmcnt(12)\n"
      "v_mfma_f32_16x16x32_bf16 v[104:107], v[40:43], v[224:227], v[104:107]\n"
      "buffer_load_dwordx4 v[244:247], v10, s[36:39], s27 offen offset:0 sc1\n"
      "v_mfma_f32_16x16x32_bf16 v[72:75], v[40:43], v[228:231], v[72:75]\n"
      "ds_read_b64_tr_b16 v[40:41], v26 offset:49152\n"
      "ds_read_b64_tr_b16 v[42:43], v26 offset:49664\n"
      "v_mfma_f32_16x16x32_bf16 v[108:111], v[44:47], v[224:227], v[108:111]\n"
      "v_mfma_f32_16x16x32_bf16 v[76:79], v[44:47], v[228:231], v[76:79]\n"
      "ds_read_b64_tr_b16 v[44:45], v27 offset:49152\n"
      "ds_read_b64_tr_b16 v[46:47], v27 offset:49664\n"
      "s_waitcnt vmcnt(11)\n"
      "s_waitcnt lgkmcnt(12)\n"
      "v_mfma_f32_16x16x32_bf16 v[96:99], v[48:51], v[232:235], v[96:99]\n"
      "buffer_load_dwordx4 v[248:251], v10, s[36:39], s27 offen offset:1024 sc1\n"
      "v_mfma_f32_16x16x32_bf16 v[64:67], v[48:51], v[236:239], v[64:67]\n"
      "ds_read_b64_tr_b16 v[48:49], v24 offset:53248\n"
      "ds_read_b64_tr_b16 v[50:51], v24 offset:53760\n"
      "v_mfma_f32_16x16x32_bf16 v[100:103], v[52:55], v[232:235], v[100:103]\n"
      "v_mfma_f32_16x16x32_bf16 v[68:71], v[52:55], v[236:239], v[68:71]\n"
      "ds_read_b64_tr_b16 v[52:53], v25 offset:53248\n"
      "ds_read_b64_tr_b16 v[54:55], v25 offset:53760\n"
      "s_waitcnt lgkmcnt(12)\n"
      "v_mfma_f32_16x16x32_bf16 v[104:107], v[56:59], v[232:235], v[104:107]\n"
      "buffer_load_dwordx4 v[252:255], v10, s[36:39], s27 offen offset:2048 sc1\n"
      "v_mfma_f32_16x16x32_bf16 v[72:75], v[56:59], v[236:239], v[72:75]\n"
      "ds_read_b64_tr_b16 v[56:57], v26 offset:53248\n"
      "ds_read_b64_tr_b16 v[58:59], v26 offset:53760\n"
      "v_mfma_f32_16x16x32_bf16 v[108:111], v[60:63], v[232:235], v[108:111]\n"
      "v_mfma_f32_16x16x32_bf16 v[76:79], v[60:63], v[236:239], v[76:79]\n"
      "ds_read_b64_tr_b16 v[60:61], v27 offset:53248\n"
      "ds_read_b64_tr_b16 v[62:63], v27 offset:53760\n"
      "buffer_load_dwordx4 v[128:131], v10, s[36:39], s27 offen offset:3072 sc1\n"
      "s_nop 2\n"
      "s_waitcnt lgkmcnt(7)\n"
      "ds_write_b128 v31, v[64:67] offset:12288\n"
      "ds_write_b128 v31, v[68:71] offset:13312\n"
      "s_add_u32 s27, s27, 4096\n"
      "buffer_load_dwordx4 v[132:135], v10, s[36:39], s27 offen offset:0 sc1\n"
      "ds_write_b128 v31, v[72:75] offset:14336\n"
      "ds_write_b128 v31, v[76:79] offset:15360\n"
      "buffer_load_dwordx4 v[136:139], v10, s[36:39], s27 offen offset:1024 sc1\n"
      "ds_read_b64_tr_b16 v[80:81], v24 offset:57344\n"
      "ds_read_b64_tr_b16 v[82:83], v24 offset:57856\n"
      "ds_read_b64_tr_b16 v[84:85], v25 offset:57344\n"
      "ds_read_b64_tr_b16 v[86:87], v25 offset:57856\n"
      "s_waitcnt lgkmcnt(7)\n"
      "ds_read_b64_tr_b16 v[88:89], v26 offset:57344\n"
      "ds_read_b64_tr_b16 v[90:91], v26 offset:57856\n"
      "ds_read_b64_tr_b16 v[92:93], v27 offset:57344\n"
      "ds_read_b64_tr_b16 v[94:95], v27 offset:57856\n"
      "buffer_load_dwordx4 v[140:143], v10, s[36:39], s27 offen offset:2048 sc1\n"
      "s_waitcnt lgkmcnt(8)\n"
      "s_barrier\n"
      "v_xor_b32 v31, 4096, v31\n"
      "ds_read_b128 v[64:67], v31 offset:12288\n"
      "ds_read_b128 v[68:71], v31 offset:13312\n"
      "ds_read_b128 v[72:75], v31 offset:14336\n"
      "ds_read_b128 v[76:79], v31 offset:15360\n"
      "buffer_load_dwordx4 v[144:147], v10, s[36:39], s27 offen offset:3072 sc1\n"
      "ds_read_b64_tr_b16 v[112:113], v24 offset:61440\n"
      "ds_read_b64_tr_b16 v[114:115], v24 offset:61952\n"
      "s_waitcnt lgkmcnt(7)\n"
      "ds_read_b64_tr_b16 v[116:117], v25 offset:61440\n"
      "ds_read_b64_tr_b16 v[118:119], v25 offset:61952\n"
      "ds_read_b64_tr_b16 v[120:121], v26 offset:61440\n"
      "ds_read_b64_tr_b16 v[122:123], v26 offset:61952\n"
      "ds_read_b64_tr_b16 v[124:125], v27 offset:61440\n"
      "ds_read_b64_tr_b16 v[126:127], v27 offset:61952\n"
      "s_add_u32 s27, s27, 4096\n"
      "buffer_load_dwordx4 v[148:151], v10, s[36:39], s27 offen offset:0 sc1\n"
      "s_waitcnt lgkmcnt(8)\n"
      "v_add_f32 v96, v96, v64\n"
      "v_add_f32 v97, v97, v65\n"
      "v_add_f32 v98, v98, v66\n"
      "v_add_f32 v99, v99, v67\n"
      "v_add_f32 v100, v100, v68\n"
      "v_add_f32 v101, v101, v69\n"
      "v_add_f32 v102, v102, v70\n"
      "v_add_f32 v103, v103, v71\n"
      "v_add_f32 v104, v104, v72\n"
      "v_add_f32 v105, v105, v73\n"
      "v_add_f32 v106, v106, v74\n"
      "v_add_f32 v107, v107, v75\n"
      "v_add_f32 v108, v108, v76\n"
      "v_add_f32 v109, v109, v77\n"
      "v_add_f32 v110, v110, v78\n"
      "v_add_f32 v111, v111, v79\n"
      "buffer_load_dwordx4 v[152:155], v10, s[36:39], s27 offen offset:1024 sc1\n"
      "v_add_u32 v24, 65536, v24\n"
      "v_add_u32 v25, 65536, v25\n"
      "v_add_u32 v26, 65536, v26\n"
      "v_add_u32 v27, 65536, v27\n"
      "ds_read_b64_tr_b16 v[176:177], v24 offset:0\n"
      "ds_read_b64_tr_b16 v[178:179], v24 offset:512\n"
      "ds_read_b64_tr_b16 v[180:181], v25 offset:0\n"
      "ds_read_b64_tr_b16 v[182:183], v25 offset:512\n"
      "ds_read_b64_tr_b16 v[184:185], v26 offset:0\n"
      "ds_read_b64_tr_b16 v[186:187], v26 offset:512\n"
      "s_waitcnt lgkmcnt(7)\n"
      "ds_read_b64_tr_b16 v[188:189], v27 offset:0\n"
      "ds_read_b64_tr_b16 v[190:191], v27 offset:512\n"
      "ds_read_b64_tr_b16 v[192:193], v24 offset:4096\n"
      "ds_read_b64_tr_b16 v[194:195], v24 offset:4608\n"
      "buffer_load_dwordx4 v[156:159], v10, s[36:39], s27 offen offset:2048 sc1\n"
      "ds_read_b64_tr_b16 v[196:197], v25 offset:4096\n"
      "ds_read_b64_tr_b16 v[198:199], v25 offset:4608\n"
      "ds_read_b64_tr_b16 v[200:201], v26 offset:4096\n"
      "ds_read_b64_tr_b16 v[202:203], v26 offset:4608\n"
      "s_waitcnt lgkmcnt(7)\n"
      "ds_read_b64_tr_b16 v[204:205], v27 offset:4096\n"
      "ds_read_b64_tr_b16 v[206:207], v27 offset:4608\n"
      "s_waitcnt vmcnt(12)\n"
      "s_barrier\n"
      "s_waitcnt vmcnt(11)\n"
      "v_mfma_f32_16x16x32_bf16 v[96:99], v[32:35], v[240:243], v[96:99]\n"
      "ds_read_b64_tr_b16 v[208:209], v24 offset:8192\n"
      "ds_read_b64_tr_b16 v[210:211], v24 offset:8704\n"
      "v_mfma_f32_16x16x32_bf16 v[100:103], v[36:39], v[240:243], v[100:103]\n"
      "ds_read_b64_tr_b16 v[212:213], v25 offset:8192\n"
      "ds_read_b64_tr_b16 v[214:215], v25 offset:8704\n"
      "buffer_load_dwordx4 v[160:163], v10, s[36:39], s27 offen offset:3072 sc1\n"
      "v_mfma_f32_16x16x32_bf16 v[104:107], v[40:43], v[240:243], v[104:107]\n"
      "ds_read_b64_tr_b16 v[216:217], v26 offset:8192\n"
      "ds_read_b64_tr_b16 v[218:219], v26 offset:8704\n"
      "v_mfma_f32_16x16x32_bf16 v[108:111], v[44:47], v[240:243], v[108:111]\n"
      "s_waitcnt lgkmcnt(7)\n"
      "ds_read_b64_tr_b16 v[220:221], v27 offset:8192\n"
      "ds_read_b64_tr_b16 v[222:223], v27 offset:8704\n"
      "s_waitcnt vmcnt(11)\n"
      "v_mfma_f32_16x16x32_bf16 v[96:99], v[48:51], v[244:247], v[96:99]\n"
      "ds_read_b64_tr_b16 v[224:225], v24 offset:12288\n"
      "ds_read_b64_tr_b16 v[226:227], v24 offset:12800\n"
      "v_mfma_f32_16x16x32_bf16 v[100:103], v[52:55], v[244:247], v[100:103]\n"
      "ds_read_b64_tr_b16 v[228:229], v25 offset:12288\n"
      "ds_read_b64_tr_b16 v[230:231], v25 offset:12800\n"
      "s_add_u32 s27, s27, 4096\n"
      "buffer_load_dwordx4 v[164:167], v10, s[36:39], s27 offen offset:0 sc1\n"
      "v_mfma_f32_16x16x32_bf16 v[104:107], v[56:59], v[244:247], v[104:107]\n"
      "ds_read_b64_tr_b16 v[232:233], v26 offset:12288\n"
      "ds_read_b64_tr_b16 v[234:235], v26 offset:12800\n"
      "v_mfma_f32_16x16x32_bf16 v[108:111], v[60:63], v[244:247], v[108:111]\n"
      "s_waitcnt lgkmcnt(7)\n"
      "ds_read_b64_tr_b16 v[236:237], v27 offset:12288\n"
      "ds_read_b64_tr_b16 v[238:239], v27 offset:12800\n"
      "s_waitcnt vmcnt(11)\n"
      "v_mfma_f32_16x16x32_bf16 v[96:99], v[80:83], v[248:251], v[96:99]\n"
      "ds_read_b64_tr_b16 v[64:65], v24 offset:16384\n"
      "ds_read_b64_tr_b16 v[66:67], v24 offset:16896\n"
      "v_mfma_f32_16x16x32_bf16 v[100:103], v[84:87], v[248:251], v[100:103]\n"
      "ds_read_b64_tr_b16 v[68:69], v25 offset:16384\n"
      "ds_read_b64_tr_b16 v[70:71], v25 offset:16896\n"
      "buffer_load_dwordx4 v[168:171], v10, s[36:39], s27 offen offset:1024 sc1\n"
      "v_mfma_f32_16x16x32_bf16 v[104:107], v[88:91], v[248:251], v[104:107]\n"
      "ds_read_b64_tr_b16 v[72:73], v26 offset:16384\n"
      "ds_read_b64_tr_b16 v[74:75], v26 offset:16896\n"
      "v_mfma_f32_16x16x32_bf16 v[108:111], v[92:95], v[248:251], v[108:111]\n"
      "s_waitcnt lgkmcnt(7)\n"
      "ds_read_b64_tr_b16 v[76:77], v27 offset:16384\n"
      "ds_read_b64_tr_b16 v[78:79], v27 offset:16896\n"
      "s_waitcnt vmcnt(11)\n"
      "v_mfma_f32_16x16x32_bf16 v[96:99], v[112:115], v[252:255], v[96:99]\n"
      "ds_read_b64_tr_b16 v[32:33], v24 offset:20480\n"
      "ds_read_b64_tr_b16 v[34:35], v24 offset:20992\n"
      "v_mfma_f32_16x16x32_bf16 v[100:103], v[116:119], v[252:255], v[100:103]\n"
      "ds_read_b64_tr_b16 v[36:37], v25 offset:20480\n"
      "ds_read_b64_tr_b16 v[38:39], v25 offset:20992\n"
      "v_mfma_f32_16x16x32_bf16 v[104:107], v[120:123], v[252:255], v[104:107]\n"
      "ds_read_b64_tr_b16 v[40:41], v26 offset:20480\n"
      "ds_read_b64_tr_b16 v[42:43], v26 offset:20992\n"
      "v_mfma_f32_16x16x32_bf16 v[108:111], v[124:127], v[252:255], v[108:111]\n"
      "s_waitcnt lgkmcnt(7)\n"
      "ds_read_b64_tr_b16 v[44:45], v27 offset:20480\n"
      "ds_read_b64_tr_b16 v[46:47], v27 offset:20992\n"
      "s_waitcnt vmcnt(10)\n"
      "v_mfma_f32_16x16x32_bf16 v[96:99], v[176:179], v[128:131], v[96:99]\n"
      "ds_read_b64_tr_b16 v[48:49], v24 offset:24576\n"
      "ds_read_b64_tr_b16 v[50:51], v24 offset:25088\n"
      "v_mfma_f32_16x16x32_bf16 v[100:103], v[180:183], v[128:131], v[100:103]\n"
      "ds_read_b64_tr_b16 v[52:53], v25 offset:24576\n"
      "ds_read_b64_tr_b16 v[54:55], v25 offset:25088\n"
      "v_mfma_f32_16x16x32_bf16 v[104:107], v[184:187], v[128:131], v[104:107]\n"
      "ds_read_b64_tr_b16 v[56:57], v26 offset:24576\n"
      "ds_read_b64_tr_b16 v[58:59], v26 offset:25088\n"
      "v_mfma_f32_16x16x32_bf16 v[108:111], v[188:191], v[128:131], v[108:111]\n"
      "s_waitcnt lgkmcnt(7)\n"
      "ds_read_b64_tr_b16 v[60:61], v27 offset:24576\n"
      "ds_read_b64_tr_b16 v[62:63], v27 offset:25088\n"
      "s_waitcnt vmcnt(9)\n"
      "v_mfma_f32_16x16x32_bf16 v[96:99], v[192:195], v[132:135], v[96:99]\n"
      "ds_read_b64_tr_b16 v[80:81], v24 offset:28672\n"
      "ds_read_b64_tr_b16 v[82:83], v24 offset:29184\n"
      "v_mfma_f32_16x16x32_bf16 v[100:103], v[196:199], v[132:135], v[100:103]\n"
      "ds_read_b64_tr_b16 v[84:85], v25 offset:28672\n"
      "ds_read_b64_tr_b16 v[86:87], v25 offset:29184\n"
      "v_mfma_f32_16x16x32_bf16 v[104:107], v[200:203], v[132:135], v[104:107]\n"
      "ds_read_b64_tr_b16 v[88:89], v26 offset:28672\n"
      "ds_read_b64_tr_b16 v[90:91], v26 offset:29184\n"
      "v_mfma_f32_16x16x32_bf16 v[108:111], v[204:207], v[132:135], v[108:111]\n"
      "s_waitcnt lgkmcnt(7)\n"
      "ds_read_b64_tr_b16 v[92:93], v27 offset:28672\n"
      "ds_read_b64_tr_b16 v[94:95], v27 offset:29184\n"
      "s_waitcnt vmcnt(8)\n"
      "v_mfma_f32_16x16x32_bf16 v[96:99], v[208:211], v[136:139], v[96:99]\n"
      "v_add_u32 v24, 4294836224, v24\n"
      "v_add_u32 v25, 4294836224, v25\n"
      "v_add_u32 v26, 4294836224, v26\n"
      "v_add_u32 v27, 4294836224, v27\n"
      "ds_read_b64_tr_b16 v[112:113], v24 offset:0\n"
      "ds_read_b64_tr_b16 v[114:115], v24 offset:512\n"
      "v_mfma_f32_16x16x32_bf16 v[100:103], v[212:215], v[136:139], v[100:103]\n"
      "ds_read_b64_tr_b16 v[116:117], v25 offset:0\n"
      "ds_read_b64_tr_b16 v[118:119], v25 offset:512\n"
      "v_mfma_f32_16x16x32_bf16 v[104:107], v[216:219], v[136:139], v[104:107]\n"
      "ds_read_b64_tr_b16 v[120:121], v26 offset:0\n"
      "ds_read_b64_tr_b16 v[122:123], v26 offset:512\n"
      "v_mfma_f32_16x16x32_bf16 v[108:111], v[220:223], v[136:139], v[108:111]\n"
      "s_waitcnt lgkmcnt(7)\n"
      "ds_read_b64_tr_b16 v[124:125], v27 offset:0\n"
      "ds_read_b64_tr_b16 v[126:127], v27 offset:512\n"
      "s_waitcnt vmcnt(7)\n"
      "v_mfma_f32_16x16x32_bf16 v[96:99], v[224:227], v[140:143], v[96:99]\n"
      "ds_read_b64_tr_b16 v[176:177], v24 offset:4096\n"
      "ds_read_b64_tr_b16 v[178:179], v24 offset:4608\n"
      "v_mfma_f32_16x16x32_bf16 v[100:103], v[228:231], v[140:143], v[100:103]\n"
      "ds_read_b64_tr_b16 v[180:181], v25 offset:4096\n"
      "ds_read_b64_tr_b16 v[182:183], v25 offset:4608\n"
      "v_mfma_f32_16x16x32_bf16 v[104:107], v[232:235], v[140:143], v[104:107]\n"
      "ds_read_b64_tr_b16 v[184:185], v26 offset:4096\n"
      "ds_read_b64_tr_b16 v[186:187], v26 offset:4608\n"
      "v_mfma_f32_16x16x32_bf16 v[108:111], v[236:239], v[140:143], v[108:111]\n"
      "s_waitcnt lgkmcnt(7)\n"
      "ds_read_b64_tr_b16 v[188:189], v27 offset:4096\n"
      "ds_read_b64_tr_b16 v[190:191], v27 offset:4608\n"
      "s_waitcnt vmcnt(6)\n"
      "v_mfma_f32_16x16x32_bf16 v[96:99], v[64:67], v[144:147], v[96:99]\n"
      "ds_read_b64_tr_b16 v[192:193], v24 offset:8192\n"
      "ds_read_b64_tr_b16 v[194:195], v24 offset:8704\n"
      "v_mfma_f32_16x16x32_bf16 v[100:103], v[68:71], v[144:147], v[100:103]\n"
      "ds_read_b64_tr_b16 v[196:197], v25 offset:8192\n"
      "ds_read_b64_tr_b16 v[198:199], v25 offset:8704\n"
      "v_mfma_f32_16x16x32_bf16 v[104:107], v[72:75], v[144:147], v[104:107]\n"
      "ds_read_b64_tr_b16 v[200:201], v26 offset:8192\n"
      "ds_read_b64_tr_b16 v[202:203], v26 offset:8704\n"
      "v_mfma_f32_16x16x32_bf16 v[108:111], v[76:79], v[144:147], v[108:111]\n"
      "s_waitcnt lgkmcnt(7)\n"
      "ds_read_b64_tr_b16 v[204:205], v27 offset:8192\n"
      "ds_read_b64_tr_b16 v[206:207], v27 offset:8704\n"
      "s_waitcnt vmcnt(5)\n"
      "v_mfma_f32_16x16x32_bf16 v[96:99], v[32:35], v[148:151], v[96:99]\n"
      "v_mfma_f32_16x16x32_bf16 v[100:103], v[36:39], v[148:151], v[100:103]\n"
      "v_mfma_f32_16x16x32_bf16 v[104:107], v[40:43], v[148:151], v[104:107]\n"
      "v_mfma_f32_16x16x32_bf16 v[108:111], v[44:47], v[148:151], v[108:111]\n"
      "s_waitcnt vmcnt(4)\n"
      "v_mfma_f32_16x16x32_bf16 v[96:99], v[48:51], v[152:155], v[96:99]\n"
      "v_mfma_f32_16x16x32_bf16 v[100:103], v[52:55], v[152:155], v[100:103]\n"
      "v_mfma_f32_16x16x32_bf16 v[104:107], v[56:59], v[152:155], v[104:107]\n"
      "v_mfma_f32_16x16x32_bf16 v[108:111], v[60:63], v[152:155], v[108:111]\n"
      "s_waitcnt vmcnt(3)\n"
      "v_mfma_f32_16x16x32_bf16 v[96:99], v[80:83], v[156:159], v[96:99]\n"
      "v_mfma_f32_16x16x32_bf16 v[100:103], v[84:87], v[156:159], v[100:103]\n"
      "v_mfma_f32_16x16x32_bf16 v[104:107], v[88:91], v[156:159], v[104:107]\n"
      "v_mfma_f32_16x16x32_bf16 v[108:111], v[92:95], v[156:159], v[108:111]\n"
      "s_waitcnt vmcnt(2)\n"
      "v_mfma_f32_16x16x32_bf16 v[96:99], v[112:115], v[160:163], v[96:99]\n"
      "v_mfma_f32_16x16x32_bf16 v[100:103], v[116:119], v[160:163], v[100:103]\n"
      "v_mfma_f32_16x16x32_bf16 v[104:107], v[120:123], v[160:163], v[104:107]\n"
      "v_mfma_f32_16x16x32_bf16 v[108:111], v[124:127], v[160:163], v[108:111]\n"
      "s_waitcnt vmcnt(1)\n"
      "v_mfma_f32_16x16x32_bf16 v[96:99], v[176:179], v[164:167], v[96:99]\n"
      "v_mfma_f32_16x16x32_bf16 v[100:103], v[180:183], v[164:167], v[100:103]\n"
      "s_waitcnt lgkmcnt(8)\n"
      "v_mfma_f32_16x16x32_bf16 v[104:107], v[184:187], v[164:167], v[104:107]\n"
      "v_mfma_f32_16x16x32_bf16 v[108:111], v[188:191], v[164:167], v[108:111]\n"
      "s_waitcnt vmcnt(0)\n"
      "s_waitcnt lgkmcnt(4)\n"
      "v_mfma_f32_16x16x32_bf16 v[96:99], v[192:195], v[168:171], v[96:99]\n"
      "v_mfma_f32_16x16x32_bf16 v[100:103], v[196:199], v[168:171], v[100:103]\n"
      "s_waitcnt lgkmcnt(0)\n"
      "v_mfma_f32_16x16x32_bf16 v[104:107], v[200:203], v[168:171], v[104:107]\n"
      "v_mfma_f32_16x16x32_bf16 v[108:111], v[204:207], v[168:171], v[108:111]\n"
      "v_xor_b32 v4, 32, v28\n"
      "v_xor_b32 v5, 64, v28\n"
      "v_xor_b32 v6, 96, v28\n"
      "s_nop 1\n"
      "v_cvt_pk_bf16_f32 v32, v96, v97\n"
      "v_cvt_pk_bf16_f32 v33, v98, v99\n"
      "ds_write_b64 v28, v[32:33]\n"
      "v_cvt_pk_bf16_f32 v34, v100, v101\n"
      "v_cvt_pk_bf16_f32 v35, v102, v103\n"
      "ds_write_b64 v4, v[34:35]\n"
      "v_cvt_pk_bf16_f32 v36, v104, v105\n"
      "v_cvt_pk_bf16_f32 v37, v106, v107\n"
      "ds_write_b64 v5, v[36:37]\n"
      "v_cvt_pk_bf16_f32 v38, v108, v109\n"
      "v_cvt_pk_bf16_f32 v39, v110, v111\n"
      "ds_write_b64 v6, v[38:39]\n"
      "ds_read_b128 v[40:43], v29\n"
      "ds_read_b128 v[44:47], v29 offset:1024\n"
      "s_waitcnt lgkmcnt(1)\n"
      "buffer_store_dwordx4 v[40:43], v15, s[44:47], s91 offen sc0 sc1\n"
      "s_waitcnt lgkmcnt(0)\n"
      "buffer_store_dwordx4 v[44:47], v15, s[44:47], s90 offen sc0 sc1\n"
      "s_endpgm\n"
      ".Lglu_CODE_END:\n"
      :
      : "s"(ka), "s"(blockIdx.x), "v"(threadIdx.x)
      : "v1", "v2", "v3", "v4", "v5", "v6", "v7", "v8", "v9", "v10", "v11", "v12", "v13", "v14", "v15", "v16", "v17", "v18", "v19", "v20", "v21", "v22", "v23", "v24", "v25", "v26", "v27", "v28", "v29", "v30", "v31", "v32", "v33", "v34", "v35", "v36", "v37", "v38", "v39", "v40", "v41", "v42", "v43", "v44", "v45", "v46", "v47", "v48", "v49", "v50", "v51", "v52", "v53", "v54", "v55", "v56", "v57", "v58", "v59", "v60", "v61", "v62", "v63", "v64", "v65", "v66", "v67", "v68", "v69", "v70", "v71", "v72", "v73", "v74", "v75", "v76", "v77", "v78", "v79", "v80", "v81", "v82", "v83", "v84", "v85", "v86", "v87", "v88", "v89", "v90", "v91", "v92", "v93", "v94", "v95", "v96", "v97", "v98", "v99", "v100", "v101", "v102", "v103", "v104", "v105", "v106", "v107", "v108", "v109", "v110", "v111", "v112", "v113", "v114", "v115", "v116", "v117", "v118", "v119", "v120", "v121", "v122", "v123", "v124", "v125", "v126", "v127", "v128", "v129", "v130", "v131", "v132", "v133", "v134", "v135", "v136", "v137", "v138", "v139", "v140", "v141", "v142", "v143", "v144", "v145", "v146", "v147", "v148", "v149", "v150", "v151", "v152", "v153", "v154", "v155", "v156", "v157", "v158", "v159", "v160", "v161", "v162", "v163", "v164", "v165", "v166", "v167", "v168", "v169", "v170", "v171", "v172", "v173", "v174", "v175", "v176", "v177", "v178", "v179", "v180", "v181", "v182", "v183", "v184", "v185", "v186", "v187", "v188", "v189", "v190", "v191", "v192", "v193", "v194", "v195", "v196", "v197", "v198", "v199", "v200", "v201", "v202", "v203", "v204", "v205", "v206", "v207", "v208", "v209", "v210", "v211", "v212", "v213", "v214", "v215", "v216", "v217", "v218", "v219", "v220", "v221", "v222", "v223", "v224", "v225", "v226", "v227", "v228", "v229", "v230", "v231", "v232", "v233", "v234", "v235", "v236", "v237", "v238", "v239", "v240", "v241", "v242", "v243", "v244", "v245", "v246", "v247", "v248", "v249", "v250", "v251", "v252", "v253", "v254", "v255", "s8", "s9", "s10", "s11", "s12", "s13", "s14", "s15", "s16", "s17", "s18", "s19", "s20", "s21", "s22", "s23", "s24", "s25", "s26", "s27", "s28", "s29", "s30", "s31", "s33", "s34", "s35", "s36", "s37", "s38", "s39", "s40", "s41", "s42", "s43", "s44", "s45", "s46", "s47", "s48", "s49", "s50", "s51", "s52", "s53", "s54", "s55", "s56", "s57", "s58", "s59", "s60", "s61", "s62", "s63", "s64", "s65", "s66", "s67", "s68", "s69", "s70", "s71", "s72", "s73", "s74", "s75", "s76", "s77", "s78", "s79", "s80", "s81", "s82", "s83", "s84", "s85", "s86", "s87", "s88", "s89", "s90", "s91", "s92", "s93", "s94", "s95", "vcc", "scc", "memory");
}

extern "C" void kernel_launch(void* const* d_in, const int* in_sizes, int n_in, void* d_out, int out_size,
                              void* d_ws, size_t ws_size, hipStream_t stream) {
  (void)hipFuncSetAttribute((const void*)glu_fused, hipFuncAttributeMaxDynamicSharedMemorySize, 163840);
  glu_fused<<<256, 512, 163840, stream>>>(d_in[0], d_in[1], d_in[2], d_in[3], d_out, d_ws);
}
```
